```python
import torch

def _softmax(x, axis=-1):
    return torch.softmax(x, dim=axis)
def _gelu(x):
    return torch.nn.functional.gelu(x, approximate="tanh")
def _silu(x):
    return torch.nn.functional.silu(x)
def _sigmoid(x):
    return torch.sigmoid(x)
def _layernorm(x, axis=-1, eps=1e-5):
    m = x.mean(dim=axis, keepdim=True)
    v = x.var(dim=axis, keepdim=True, unbiased=False)
    return (x - m) / torch.sqrt(v + eps)
def _scatter_add(tgt, idx, src, axis=0):
    return tgt.clone().index_add_(axis, idx, src)
def _segment_sum(src, seg, n):
    out = torch.zeros((n,) + src.shape[1:], dtype=src.dtype)
    return out.index_add_(0, seg, src)
def _pad(x, pw):
    flat = tuple(v for pair in reversed(pw) for v in pair)
    return torch.nn.functional.pad(x, flat)

kernel_name = 'concat_heads__mha_block_cached__mha_block_cached__near_ridge'

def setup_inputs():

    _w = (lambda a: (lambda t: t.to(torch.bfloat16) if t.is_floating_point() else t)(torch.as_tensor(a)))
    def _A_setup():
        B, Sq, D = 32, 64, 512
        H, Sk, Dh = 32, 512, 128
        return {
            "x": _w((lambda *s: torch.randn(*s, dtype=torch.bfloat16) * 0.8)(B, Sq, D)),
            "k_cache": _w((lambda *s: torch.randn(*s, dtype=torch.bfloat16) * 0.8)(B, H, Sk, Dh)),
            "v_cache": _w((lambda *s: torch.randn(*s, dtype=torch.bfloat16) * 0.05)(B, H, Sk, Dh)),
            "wq": _w((lambda *s: torch.randn(*s, dtype=torch.bfloat16) * 0.8)(D, H * Dh) * (D ** -0.5)),
            "wo": _w((lambda *s: torch.randn(*s, dtype=torch.bfloat16) * 0.05)(H * Dh, D) * ((H * Dh) ** -0.5)),
        }
    _A_in = _A_setup()

    def _B_setup():
        B, Sq, D = 32, 64, 512
        H, Sk, Dh = 32, 512, 128
        return {
            "x": _w((lambda *s: torch.randn(*s, dtype=torch.bfloat16) * 0.8)(B, Sq, D)),
            "k_cache": _w((lambda *s: torch.randn(*s, dtype=torch.bfloat16) * 0.8)(B, H, Sk, Dh)),
            "v_cache": _w((lambda *s: torch.randn(*s, dtype=torch.bfloat16) * 0.05)(B, H, Sk, Dh)),
            "wq": _w((lambda *s: torch.randn(*s, dtype=torch.bfloat16) * 0.8)(D, H * Dh) * (D ** -0.5)),
            "wo": _w((lambda *s: torch.randn(*s, dtype=torch.bfloat16) * 0.05)(H * Dh, D) * ((H * Dh) ** -0.5)),
        }
    _B_in = _B_setup()

    return {
        "A_x": _A_in["x"],
        "A_k_cache": _A_in["k_cache"],
        "A_v_cache": _A_in["v_cache"],
        "A_wq": _A_in["wq"],
        "A_wo": _A_in["wo"],
        "B_x": _B_in["x"],
        "B_k_cache": _B_in["k_cache"],
        "B_v_cache": _B_in["v_cache"],
        "B_wq": _B_in["wq"],
        "B_wo": _B_in["wo"],
    }

def reference(A_x, A_k_cache, A_v_cache, A_wq, A_wo, B_x, B_k_cache, B_v_cache, B_wq, B_wo):
    def _A_ref(x, k_cache, v_cache, wq, wo):
        B, Sq, D = x.shape
        H, Dh = 32, 128
        q = (lambda x, *d: x.permute(*d))((x @ wq).reshape(B, Sq, H, Dh), 0, 2, 1, 3)
        scores = torch.einsum("bhqd,bhkd->bhqk", q, k_cache) * (Dh ** -0.5)
        p = _softmax(scores, axis=-1)
        o = torch.einsum("bhqk,bhkd->bhqd", p, v_cache)
        return (lambda x, *d: x.permute(*d))(o, 0, 2, 1, 3).reshape(B, Sq, H * Dh) @ wo

    def _B_ref(x, k_cache, v_cache, wq, wo):
        B, Sq, D = x.shape
        H, Dh = 32, 128
        q = (lambda x, *d: x.permute(*d))((x @ wq).reshape(B, Sq, H, Dh), 0, 2, 1, 3)
        scores = torch.einsum("bhqd,bhkd->bhqk", q, k_cache) * (Dh ** -0.5)
        p = _softmax(scores, axis=-1)
        o = torch.einsum("bhqk,bhkd->bhqd", p, v_cache)
        return (lambda x, *d: x.permute(*d))(o, 0, 2, 1, 3).reshape(B, Sq, H * Dh) @ wo

    a = _A_ref(A_x, A_k_cache, A_v_cache, A_wq, A_wo)
    b = _B_ref(B_x, B_k_cache, B_v_cache, B_wq, B_wo)
    return torch.concatenate([a, b], axis=-1)
```

```cpp
#include <hip/hip_runtime.h>
#include <hip/hip_bf16.h>

typedef __amdgpu_buffer_rsrc_t rsrc_t;
using bf16x8 = __attribute__((ext_vector_type(8))) short;
using s16x4 = __attribute__((ext_vector_type(4))) short;
using f32x4 = __attribute__((ext_vector_type(4))) float;
using f32x2 = __attribute__((ext_vector_type(2))) float;
using u32x4 = __attribute__((ext_vector_type(4))) unsigned;
using u32x2 = __attribute__((ext_vector_type(2))) unsigned;
using bf16x2_t = __attribute__((ext_vector_type(2))) __bf16;
typedef short v4i16_t __attribute__((ext_vector_type(4)));
#define LAS __attribute__((address_space(3)))
#define DI __device__ __forceinline__

constexpr int STAGE = 16384, TILE = 65536, NSTG = 4;
constexpr int PB = 2 * TILE;
constexpr int LDS_BYTES = PB + 4 * 2560;
constexpr float SC = 0.08838834764831845f * 1.4426950408889634f;

DI unsigned rfl(unsigned x) { return __builtin_amdgcn_readfirstlane(x); }
DI unsigned cvtpk(float lo, float hi) { f32x2 v = {lo, hi}; return __builtin_bit_cast(unsigned, __builtin_convertvector(v, bf16x2_t)); }
DI f32x4 mfma(bf16x8 a, bf16x8 b, f32x4 c) { return __builtin_amdgcn_mfma_f32_16x16x32_bf16(a, b, c, 0, 0, 0); }
DI s16x4 trd(unsigned addr) { return __builtin_bit_cast(s16x4, __builtin_amdgcn_ds_read_tr16_b64_v4i16((LAS v4i16_t*)(size_t)addr)); }
DI bf16x8 ldsr128(unsigned addr) { return *(const LAS bf16x8*)(size_t)addr; }
DI bf16x8 cat(s16x4 a, s16x4 b) { return (bf16x8){a[0], a[1], a[2], a[3], b[0], b[1], b[2], b[3]}; }
DI bf16x8 pack8(f32x4 a, f32x4 b) { u32x4 w = {cvtpk(a[0], a[1]), cvtpk(a[2], a[3]), cvtpk(b[0], b[1]), cvtpk(b[2], b[3])}; return __builtin_bit_cast(bf16x8, w); }

template <int N> DI void waitbar() { asm volatile("s_waitcnt vmcnt(%0) lgkmcnt(0)\n\ts_barrier" ::"i"(N) : "memory"); }
DI void waitbar_n(int n) { if (n == 2) waitbar<8>(); else if (n == 1) waitbar<4>(); else waitbar<0>(); }

#define DMA4(POLS)                                                                                            \
  asm volatile("s_mov_b32 m0, %0\n\ts_nop 0\n\t"                                                              \
               "buffer_load_dwordx4 %1, %5, %6 offen " POLS " lds\n\t"                                        \
               "buffer_load_dwordx4 %2, %5, %7 offen offset:1024 " POLS " lds\n\t"                            \
               "buffer_load_dwordx4 %3, %5, %8 offen offset:2048 " POLS " lds\n\t"                            \
               "buffer_load_dwordx4 %4, %5, %9 offen offset:3072 " POLS " lds" ::"s"(m0),                     \
               "v"(v0), "v"(v1), "v"(v2), "v"(v3), "s"(srd), "s"(s0), "s"(s1), "s"(s2), "s"(s3)               \
               : "m0", "memory")
template <bool NT>
DI void dma4(unsigned m0, unsigned v0, unsigned v1, unsigned v2, unsigned v3, rsrc_t srd, unsigned s0, unsigned s1, unsigned s2, unsigned s3) {
  if constexpr (NT) DMA4("nt"); else DMA4("");
}

#define SBAR() __builtin_amdgcn_sched_barrier(0)
#define PIN(x) asm volatile("" : "+v"(x))
DI float max3f(float a, float b, float c) { float r; asm("v_max3_f32 %0, %1, %2, %3" : "=v"(r) : "v"(a), "v"(b), "v"(c)); return r; }
DI float max2f(float a, float b) { float r; asm("v_max_f32_e32 %0, %1, %2" : "=v"(r) : "v"(a), "v"(b)); return r; }
struct NoFill { DI void operator()(int) const {} };
template <bool ZERO, int AH = 3, class F = NoFill>
DI void tile_T(f32x4* acc, unsigned tb, unsigned off, bf16x8 b0, bf16x8 b1, F fill = F()) {
  s16x4 fr[8][4];
#pragma unroll
  for (int c = 0; c < 8 + AH; ++c) {
    if (c < 8) {
      const unsigned a = (tb ^ (32u * c)) + off;
      fr[c][0] = trd(a); fr[c][1] = trd(a + 4096); fr[c][2] = trd(a + 8192); fr[c][3] = trd(a + 12288);
    }
    if (c >= AH) {
      const int ct = c - AH;
      f32x4 cc = ZERO ? (f32x4){0.f, 0.f, 0.f, 0.f} : acc[ct];
      cc = mfma(cat(fr[ct][0], fr[ct][1]), b0, cc);
      acc[ct] = mfma(cat(fr[ct][2], fr[ct][3]), b1, cc);
      fill(ct);
    }
    SBAR();
  }
}

DI float red_max(float v) {
  auto r = __builtin_amdgcn_permlane16_swap(__float_as_uint(v), __float_as_uint(v), false, false);
  v = max2f(__uint_as_float(r[0]), __uint_as_float(r[1]));
  auto q = __builtin_amdgcn_permlane32_swap(__float_as_uint(v), __float_as_uint(v), false, false);
  return max2f(__uint_as_float(q[0]), __uint_as_float(q[1]));
}
DI float red_sum(float v) {
  auto r = __builtin_amdgcn_permlane16_swap(__float_as_uint(v), __float_as_uint(v), false, false);
  v = __uint_as_float(r[0]) + __uint_as_float(r[1]);
  auto q = __builtin_amdgcn_permlane32_swap(__float_as_uint(v), __float_as_uint(v), false, false);
  return __uint_as_float(q[0]) + __uint_as_float(q[1]);
}

__global__ __launch_bounds__(512, 2) void mha_fused(const char* __restrict__ xA, const char* __restrict__ kA, const char* __restrict__ vA,
                                                    const char* __restrict__ wqA, const char* __restrict__ woA, const char* __restrict__ xB,
                                                    const char* __restrict__ kB, const char* __restrict__ vB, const char* __restrict__ wqB,
                                                    const char* __restrict__ woB, float* __restrict__ part) {
  extern __shared__ __attribute__((aligned(16))) char lds[];
  const unsigned tid = threadIdx.x, lane = tid & 63, wid = rfl(tid >> 6), w4 = wid & 3;
  const unsigned xg = blockIdx.x & 7, p = xg >> 2, hg = xg & 3, b = blockIdx.x >> 3;
  const unsigned rot = b >> 3;
  const char* kc = (p ? kB : kA) + (size_t)(b * 32 + hg * 8) * 131072;
  const char* vc = (p ? vB : vA) + (size_t)(b * 32 + hg * 8) * 131072;
  const char* wq = p ? wqB : wqA;
  const char* wo = p ? woB : woA;
  const char* xp = (p ? xB : xA) + (size_t)b * 65536;
  const rsrc_t sK = __builtin_amdgcn_make_buffer_rsrc((void*)kc, (short)0, 1 << 20, 0x00020000);
  const rsrc_t sV = __builtin_amdgcn_make_buffer_rsrc((void*)vc, (short)0, 1 << 20, 0x00020000);
  const rsrc_t sQ = __builtin_amdgcn_make_buffer_rsrc((void*)wq, (short)0, 1 << 22, 0x00020000);
  const rsrc_t sO = __builtin_amdgcn_make_buffer_rsrc((void*)wo, (short)0, 1 << 22, 0x00020000);
  const unsigned ldsb = (unsigned)(size_t)lds;
  const unsigned r = lane & 15, g = lane >> 4;
  const unsigned swT = 16 * (r ^ (2 * g));
  const unsigned dq = g * 8192 + swT, dO = g * 1024 + swT;
  const unsigned tb0 = ldsb + (lane >> 2) * 256 + 32 * ((lane >> 2) & 7) + 8 * (lane & 3);
  const unsigned pbuf = ldsb + PB + w4 * 2560;

  auto head = [&](int u) -> unsigned { return (unsigned)(u + b) & 7u; };
  auto dmaWq = [&](int u, int t, int slot, int half) {
    const unsigned m0 = ldsb + slot * STAGE + half * TILE + w4 * 4096;
    const unsigned so = (64 * ((t + 2 * rot) & 7) + 16 * w4) * 8192 + (8 * hg + head(u)) * 256;
    dma4<false>(m0, dq, dq ^ 128, dq, dq ^ 128, sQ, so, so + 4 * 8192 - 1024, so + 8 * 8192 - 2048, so + 12 * 8192 - 3072);
  };
  auto dmaWo = [&](int u, int t, int slot, int half) {
    const unsigned m0 = ldsb + slot * STAGE + half * TILE + w4 * 4096;
    const unsigned so = ((8 * hg + head(u)) * 128 + 64 * (t & 1) + 16 * w4) * 1024 + (((t >> 1) + rot + hg) & 3) * 256;
    dma4<false>(m0, dO, dO ^ 128, dO, dO ^ 128, sO, so, so + 4 * 1024 - 1024, so + 8 * 1024 - 2048, so + 12 * 1024 - 3072);
  };

  if (wid < 4) {
    const unsigned dK = g * 256 + 16 * (r ^ g);
    auto dmaK = [&](int u, int t, int slot) {
      const unsigned m0 = ldsb + slot * STAGE + w4 * 4096;
      const unsigned so = head(u) * 131072 + t * 16384 + w4 * 4096;
      dma4<true>(m0, dK, dK ^ 64, dK ^ 128, dK ^ 192, sK, so, so, so, so);
    };
    bf16x8 xf[16];
    {
      const char* xr = xp + (16 * w4 + r) * 1024 + 8 * g;
#pragma unroll
      for (int i = 0; i < 8; ++i) {
        const unsigned kk = (i + 2 * rot) & 7;
#pragma unroll
        for (int m = 0; m < 2; ++m) {
          const u32x2 lo = *(const u32x2*)(xr + kk * 128 + m * 64);
          const u32x2 hi = *(const u32x2*)(xr + kk * 128 + m * 64 + 32);
          xf[2 * i + m] = __builtin_bit_cast(bf16x8, (u32x4){lo[0], lo[1], hi[0], hi[1]});
        }
      }
    }
    dmaWq(0, 0, 0, 0); dmaWq(0, 2, 1, 0); dmaWq(0, 4, 2, 0);
    const unsigned sig = ((g & 1) << 1) | (g >> 1);
    const unsigned kb0 = ldsb + r * 256 + 16 * (sig ^ r);
    f32x4 qacc[8];
    bf16x8 qf[4];
    auto packQ = [&]() {
#pragma unroll
      for (int m = 0; m < 4; ++m) {
        const unsigned w0 = cvtpk(qacc[2 * m][0], qacc[2 * m][1]), w1 = cvtpk(qacc[2 * m][2], qacc[2 * m][3]);
        const unsigned w2 = cvtpk(qacc[2 * m + 1][0], qacc[2 * m + 1][1]), w3 = cvtpk(qacc[2 * m + 1][2], qacc[2 * m + 1][3]);
        auto s0 = __builtin_amdgcn_permlane16_swap(w0, w2, false, false);
        auto s1 = __builtin_amdgcn_permlane16_swap(w1, w3, false, false);
        qf[m] = __builtin_bit_cast(bf16x8, (u32x4){s0[0], s1[0], s0[1], s1[1]});
      }
    };
    waitbar<8>();
#pragma unroll
    for (int i = 0; i < 4; ++i) {
      if (i == 0) dmaWq(0, 6, 3, 0); else if (i == 2) dmaWq(1, 0, 1, 0); else dmaK(0, i >> 1, (i + 3) & 3);
      if (i == 0) tile_T<true>(qacc, tb0, i * STAGE, xf[4 * i], xf[4 * i + 1]);
      else tile_T<false>(qacc, tb0, i * STAGE, xf[4 * i], xf[4 * i + 1]);
      tile_T<false>(qacc, tb0 + TILE, i * STAGE, xf[4 * i + 2], xf[4 * i + 3]);
      if (i == 3) packQ();
      waitbar<8>();
    }
    float mrun = 0.f, lsum = 0.f;
    for (int u = 0; u < 8; ++u) {
#pragma unroll
      for (int t = 0; t < 8; ++t) {
        {
          const int un = t < 7 ? u : u + 1, tn = (t + 1) & 7;
          if (un < 7) dmaWq(un + 1, tn, (2 * t + 3) & 3, 0);
        }
        f32x4 s[4];
        float mx;
        {
          bf16x8 kf[4][4];
#pragma unroll
          for (int c = 0; c < 6; ++c) {
            if (c < 4) {
#pragma unroll
              for (int ks = 0; ks < 4; ++ks) kf[c][ks] = ldsr128((kb0 ^ (64u * ks)) + (((2 * t) & 3) * STAGE + 4096 * c));
            }
            if (c >= 2) {
              const int kt = c - 2;
              f32x4 cc = {0.f, 0.f, 0.f, 0.f};
#pragma unroll
              for (int ks = 0; ks < 4; ++ks) cc = mfma(kf[kt][ks], qf[ks], cc);
              s[kt] = cc;
              if (kt == 1) mx = max2f(max3f(s[0][0], s[0][1], s[0][2]), s[0][3]);
              if (kt >= 2) mx = max3f(max3f(mx, s[kt - 1][0], s[kt - 1][1]), s[kt - 1][2], s[kt - 1][3]);
            }
            SBAR();
          }
          mx = max3f(max3f(mx, s[3][0], s[3][1]), s[3][2], s[3][3]);
        }
        mx = red_max(mx);
        float alpha = 0.f;
        if (t == 0) { mrun = mx; }
        else { const float mn = max2f(mrun, mx); alpha = __builtin_amdgcn_exp2f((mrun - mn) * SC); mrun = mn; }
        {
          int n;
          if (t < 7) n = 1 + (u < 7); else n = (u < 7) + (u < 6);
          waitbar_n(n);
        }
        {
          const int un = t < 6 ? u : u + 1, tn = (t + 2) & 7;
          if (un < 8) dmaK(un, tn, (2 * t + 4) & 3);
          else if (t == 7) dmaWo(7, 0, (2 * t + 4) & 3, 0);
        }
        {
          const float nm = -mrun * SC;
          float ps = 0.f;
          auto fill = [&](int ct) {
            s[ct >> 1][2 * (ct & 1)] = __builtin_amdgcn_exp2f(fmaf(s[ct >> 1][2 * (ct & 1)], SC, nm));
            s[ct >> 1][2 * (ct & 1) + 1] = __builtin_amdgcn_exp2f(fmaf(s[ct >> 1][2 * (ct & 1) + 1], SC, nm));
            ps += s[ct >> 1][2 * (ct & 1)] + s[ct >> 1][2 * (ct & 1) + 1];
            PIN(ps);
          };
          if (t == 0) tile_T<true, 3>(qacc, tb0, ((2 * t + 1) & 3) * STAGE, xf[2 * t], xf[2 * t + 1], fill);
          else tile_T<false, 3>(qacc, tb0, ((2 * t + 1) & 3) * STAGE, xf[2 * t], xf[2 * t + 1], fill);
          lsum = t == 0 ? ps : fmaf(lsum, alpha, ps);
          PIN(lsum);
          *(LAS bf16x8*)(size_t)(pbuf + lane * 16) = pack8(s[0], s[1]);
          *(LAS bf16x8*)(size_t)(pbuf + 1024 + lane * 16) = pack8(s[2], s[3]);
          *(LAS float*)(size_t)(pbuf + 2048 + lane * 4) = alpha;
          if (t == 7) *(LAS float*)(size_t)(pbuf + 2304 + lane * 4) = 1.0f / red_sum(lsum);
        }
        if (t == 7) packQ();
        {
          int n;
          if (t < 6) n = (u < 7) + 1;
          else if (t == 6) n = (u < 7) + (u < 7);
          else n = (u < 6) + 1;
          waitbar_n(n);
        }
      }
    }
    dmaWo(7, 2, 3, 0); waitbar<8>();
    dmaWo(7, 4, 0, 0); waitbar<8>();
    dmaWo(7, 6, 1, 0); waitbar<8>();
    waitbar<4>();
    waitbar<0>();
  } else {
    const unsigned dV = g * 256 + swT;
    const unsigned tb1 = tb0 + TILE;
    auto dmaV = [&](int u, int t, int slot) {
      const unsigned m0 = ldsb + slot * STAGE + TILE + w4 * 4096;
      const unsigned so = head(u) * 131072 + t * 16384 + w4 * 4096;
      dma4<true>(m0, dV, dV ^ 128, dV, dV ^ 128, sV, so, so, so, so);
    };
    dmaWq(0, 1, 0, 1); dmaWq(0, 3, 1, 1); dmaWq(0, 5, 2, 1);
    f32x4 oacc[32];
#pragma unroll
    for (int i = 0; i < 32; ++i) oacc[i] = (f32x4){0.f, 0.f, 0.f, 0.f};
    f32x4 o[8];
    bf16x8 of[4];
#pragma unroll
    for (int i = 0; i < 4; ++i) of[i] = (bf16x8){0, 0, 0, 0, 0, 0, 0, 0};
    waitbar<8>();
    dmaWq(0, 7, 3, 1); waitbar<8>();
    waitbar<4>();
    waitbar<0>();
    waitbar<0>();
    dmaV(0, 0, 3); waitbar<4>();
    waitbar<4>();
    for (int u = 0; u < 8; ++u) {
#pragma unroll
      for (int t = 0; t < 8; ++t) {
        {
          const int un = t < 7 ? u : u + 1, tn = (t + 1) & 7;
          if (un < 8) dmaV(un, tn, (2 * t + 5) & 3);
          else dmaWo(7, 3, (2 * t + 5) & 3, 1);
        }
        const bf16x8 p0 = ldsr128(pbuf + lane * 16), p1 = ldsr128(pbuf + 1024 + lane * 16);
        const float alpha = *(const LAS float*)(size_t)(pbuf + 2048 + lane * 4);
        float linv = 0.f;
        if (t == 7) linv = *(const LAS float*)(size_t)(pbuf + 2304 + lane * 4);
        if (u > 0) tile_T<false>(oacc + 8 * (t >> 1), tb1, ((2 * t + 2) & 3) * STAGE, of[2 * (t & 1)], of[2 * (t & 1) + 1]);
        {
          int n;
          if (t < 7) n = (u > 0) + 1; else n = 2;
          waitbar_n(n);
        }
        {
          const int un = t < 6 ? u : u + 1, tn = (t + 2) & 7;
          if (un == 8) dmaWo(7, 4 * tn + 1, (2 * t + 6) & 3, 1);
          else if (un > 0) dmaWo(un - 1, tn, (2 * t + 6) & 3, 1);
        }
        if (t == 0) tile_T<true>(o, tb1, ((2 * t + 3) & 3) * STAGE, p0, p1);
        else {
#pragma unroll
          for (int i = 0; i < 8; ++i) o[i] *= alpha;
          tile_T<false>(o, tb1, ((2 * t + 3) & 3) * STAGE, p0, p1);
        }
        if (t == 7) {
#pragma unroll
          for (int m = 0; m < 4; ++m) of[m] = pack8(o[2 * m] * linv, o[2 * m + 1] * linv);
        }
        {
          int n;
          if (t < 6) n = 1 + (u > 0); else n = 2;
          waitbar_n(n);
        }
      }
    }
#pragma unroll
    for (int j = 0; j < 4; ++j) {
      if (j == 0) dmaWo(7, 7, 1, 1);
      tile_T<false>(oacc + 8 * j, tb0, ((2 + j) & 3) * STAGE, of[0], of[1]);
      tile_T<false>(oacc + 8 * j, tb1, ((2 + j) & 3) * STAGE, of[2], of[3]);
      if (j == 0) waitbar<8>(); else if (j == 1) waitbar<4>(); else if (j == 2) waitbar<0>();
    }
    float* pw = part + ((size_t)blockIdx.x * 4 + w4) * 8192 + lane * 4;
#pragma unroll
    for (int i = 0; i < 4; ++i) {
      const unsigned q = (i + rot + hg) & 3;
#pragma unroll
      for (int ct = 0; ct < 8; ++ct) *(f32x4*)(pw + (q * 8 + ct) * 256) = oacc[8 * i + ct];
    }
  }
}

__global__ __launch_bounds__(256) void reduce_k(const float* __restrict__ part, unsigned short* __restrict__ out) {
  const unsigned idx = blockIdx.x * 256 + threadIdx.x;
  const unsigned lane = idx & 63, nt = (idx >> 6) & 31, w4 = (idx >> 11) & 3, b = (idx >> 13) & 31, p = idx >> 18;
  f32x4 s = {0.f, 0.f, 0.f, 0.f};
#pragma unroll
  for (int hg = 0; hg < 4; ++hg) {
    const unsigned blk = b * 8 + p * 4 + hg;
    s += *(const f32x4*)(part + ((size_t)blk * 4 + w4) * 8192 + nt * 256 + lane * 4);
  }
  const unsigned q = 16 * w4 + (lane & 15), n = 16 * nt + 4 * (lane >> 4);
  u32x2 w = {cvtpk(s[0], s[1]), cvtpk(s[2], s[3])};
  *(u32x2*)(out + ((size_t)b * 64 + q) * 1024 + 512 * p + n) = w;
}

extern "C" void kernel_launch(void* const* d_in, const int* in_sizes, int n_in, void* d_out, int out_size, void* d_ws, size_t ws_size,
                              hipStream_t stream) {
  (void)in_sizes; (void)n_in; (void)out_size; (void)ws_size;
  (void)hipFuncSetAttribute((const void*)mha_fused, hipFuncAttributeMaxDynamicSharedMemorySize, LDS_BYTES);
  float* part = (float*)d_ws;
  mha_fused<<<256, 512, LDS_BYTES, stream>>>((const char*)d_in[0], (const char*)d_in[1], (const char*)d_in[2], (const char*)d_in[3],
                                             (const char*)d_in[4], (const char*)d_in[5], (const char*)d_in[6], (const char*)d_in[7],
                                             (const char*)d_in[8], (const char*)d_in[9], part);
  reduce_k<<<2048, 256, 0, stream>>>(part, (unsigned short*)d_out);
}
```

```cpp
#include <hip/hip_runtime.h>
#include <hip/hip_bf16.h>

typedef __amdgpu_buffer_rsrc_t rsrc_t;
using bf16x8 = __attribute__((ext_vector_type(8))) short;
using s16x4 = __attribute__((ext_vector_type(4))) short;
using f32x4 = __attribute__((ext_vector_type(4))) float;
using f32x16 = __attribute__((ext_vector_type(16))) float;
using f32x2 = __attribute__((ext_vector_type(2))) float;
using u32x4 = __attribute__((ext_vector_type(4))) unsigned;
using bf16x2_t = __attribute__((ext_vector_type(2))) __bf16;
typedef short v4i16_t __attribute__((ext_vector_type(4)));
#define LAS __attribute__((address_space(3)))
#define GAS __attribute__((address_space(1)))
#define DI __device__ __forceinline__
#define SBAR() __builtin_amdgcn_sched_barrier(0)
#define PIN(x) asm volatile("" : "+v"(x))

constexpr int SLOT = 8192, REG = 32768;
constexpr int XB = 4 * REG;
constexpr int PB = XB + 2 * 10240;
constexpr int LDS_BYTES = PB + 4 * 3072;
constexpr unsigned WS_FLAG = 32u << 20, WS_EPOCH = 33u << 20;
constexpr float SC = 0.08838834764831845f * 1.4426950408889634f;
constexpr unsigned OOR = 0x80000000u;
#define ABL_KV 1
#define ABL_W 1

DI unsigned rfl(unsigned x) { return __builtin_amdgcn_readfirstlane(x); }
DI unsigned cvtpk(float lo, float hi) { f32x2 v = {lo, hi}; return __builtin_bit_cast(unsigned, __builtin_convertvector(v, bf16x2_t)); }
DI f32x16 mfma(bf16x8 a, bf16x8 b, f32x16 c) { return __builtin_amdgcn_mfma_f32_32x32x16_bf16(a, b, c, 0, 0, 0); }
DI s16x4 trd(unsigned addr) { return __builtin_bit_cast(s16x4, __builtin_amdgcn_ds_read_tr16_b64_v4i16((LAS v4i16_t*)(size_t)addr)); }
DI bf16x8 ldsr128(unsigned addr) { return *(const LAS bf16x8*)(size_t)addr; }
DI void ldsw128(unsigned addr, u32x4 v) { *(LAS u32x4*)(size_t)addr = v; }
DI bf16x8 cat(s16x4 a, s16x4 b) { return (bf16x8){a[0], a[1], a[2], a[3], b[0], b[1], b[2], b[3]}; }
DI f32x16 zero16() { return (f32x16){0.f, 0.f, 0.f, 0.f, 0.f, 0.f, 0.f, 0.f, 0.f, 0.f, 0.f, 0.f, 0.f, 0.f, 0.f, 0.f}; }
DI u32x4 packacc(const f32x16& a, int s, float f) {
  return (u32x4){cvtpk(a[8 * s] * f, a[8 * s + 1] * f), cvtpk(a[8 * s + 2] * f, a[8 * s + 3] * f), cvtpk(a[8 * s + 4] * f, a[8 * s + 5] * f), cvtpk(a[8 * s + 6] * f, a[8 * s + 7] * f)};
}
DI u32x4 packacc1(const f32x16& a, int s) {
  return (u32x4){cvtpk(a[8 * s], a[8 * s + 1]), cvtpk(a[8 * s + 2], a[8 * s + 3]), cvtpk(a[8 * s + 4], a[8 * s + 5]), cvtpk(a[8 * s + 6], a[8 * s + 7])};
}
DI u32x4 swap32(u32x4 w) {
  auto s0 = __builtin_amdgcn_permlane32_swap(w[0], w[2], false, false);
  auto s1 = __builtin_amdgcn_permlane32_swap(w[1], w[3], false, false);
  return (u32x4){s0[0], s1[0], s0[1], s1[1]};
}
DI float max3f(float a, float b, float c) { return __builtin_fmaxf(__builtin_fmaxf(a, b), c); }
DI float red_max(float v) {
  auto q = __builtin_amdgcn_permlane32_swap(__float_as_uint(v), __float_as_uint(v), false, false);
  return __builtin_fmaxf(__uint_as_float(q[0]), __uint_as_float(q[1]));
}
DI float red_sum(float v) {
  auto q = __builtin_amdgcn_permlane32_swap(__float_as_uint(v), __float_as_uint(v), false, false);
  return __uint_as_float(q[0]) + __uint_as_float(q[1]);
}
template <int N> DI void waitbar() { asm volatile("s_waitcnt vmcnt(%0) lgkmcnt(0)\n\ts_barrier" ::"i"(N) : "memory"); SBAR(); }

struct Dma { rsrc_t srd; unsigned m0, so, sstep, v0; bool nt, k; };
template <int J> DI void dma_piece(const Dma& d) {
  const unsigned v = d.k ? (d.v0 ^ (64u * J)) : d.v0;
  const unsigned so = d.so + J * d.sstep;
  if (d.nt) asm volatile("s_mov_b32 m0, %0\n\ts_nop 0\n\tbuffer_load_dwordx4 %1, %2, %3 offen offset:%4 nt lds" ::"s"(d.m0), "v"(v), "s"(d.srd), "s"(so), "i"(1024 * J) : "m0");
  else asm volatile("s_mov_b32 m0, %0\n\ts_nop 0\n\tbuffer_load_dwordx4 %1, %2, %3 offen offset:%4 lds" ::"s"(d.m0), "v"(v), "s"(d.srd), "s"(so), "i"(1024 * J) : "m0");
}
DI void dma_all(const Dma& d) { dma_piece<0>(d); dma_piece<1>(d); dma_piece<2>(d); dma_piece<3>(d); }
template <int K0, int KS> DI void dma_slot(const Dma& d, int k) {
  if (k == K0) dma_piece<0>(d);
  if (k == K0 + KS) dma_piece<1>(d);
  if (k == K0 + 2 * KS) dma_piece<2>(d);
  if (k == K0 + 3 * KS) dma_piece<3>(d);
}

template <int N, int D, int A, class RD, class MP, class MC, class FL>
DI void pstep(RD rd, MP mprev, MC mcur, FL fill) {
#pragma unroll
  for (int k = 0; k < A; ++k) rd(k);
  SBAR();
#pragma unroll
  for (int k = 0; k < N; ++k) {
    if (k < D) mprev(N - D + k); else mcur(k - D);
    SBAR();
    if (k + A < N) rd(k + A);
    fill(k);
    SBAR();
  }
}

#define STAMP(i)

__global__ __launch_bounds__(512, 2) void mha_fused(const char* __restrict__ xA, const char* __restrict__ kA, const char* __restrict__ vA,
                                                    const char* __restrict__ wqA, const char* __restrict__ woA, const char* __restrict__ xB,
                                                    const char* __restrict__ kB, const char* __restrict__ vB, const char* __restrict__ wqB,
                                                    const char* __restrict__ woB, char* __restrict__ ws, char* __restrict__ out) {
  extern __shared__ __attribute__((aligned(16))) char lds[];
  const unsigned tid = threadIdx.x, lane = tid & 63, wid = rfl(tid >> 6), role = wid >> 1, qh = wid & 1;
  const unsigned xg = blockIdx.x & 7, p = xg >> 2, hg = xg & 3, b = blockIdx.x >> 3;
  const unsigned rot = b >> 3;
  const char* kc = (p ? kB : kA) + (size_t)(b * 32 + hg * 8) * 131072;
  const char* vc = (p ? vB : vA) + (size_t)(b * 32 + hg * 8) * 131072;
  const char* wq = p ? wqB : wqA;
  const char* wo = p ? woB : woA;
  const char* xp = (p ? xB : xA) + (size_t)b * 65536;
  const rsrc_t sK = __builtin_amdgcn_make_buffer_rsrc((void*)kc, (short)0, ABL_KV << 20, 0x00020000);
  const rsrc_t sV = __builtin_amdgcn_make_buffer_rsrc((void*)vc, (short)0, ABL_KV << 20, 0x00020000);
  const rsrc_t sQ = __builtin_amdgcn_make_buffer_rsrc((void*)wq, (short)0, ABL_W << 22, 0x00020000);
  const rsrc_t sO = __builtin_amdgcn_make_buffer_rsrc((void*)wo, (short)0, ABL_W << 22, 0x00020000);
  const rsrc_t sX = __builtin_amdgcn_make_buffer_rsrc((void*)xp, (short)0, 65536, 0x00020000);
  const unsigned ldsb = (unsigned)(size_t)lds;
  const unsigned g = lane >> 4, r16 = lane & 15, r32 = lane & 31, hh = lane >> 5;
  STAMP(0);
  const unsigned swT = 16 * (r16 ^ (4 * g));
  const unsigned dq = g * 8192 + swT, dO = g * 1024 + swT;
  const unsigned myreg = ldsb + role * REG + qh * 4096;
  auto head = [&](int u) -> unsigned { return (unsigned)(u + b) & 7u; };
  auto mkWq = [&](int u, int i, int slot, bool nt = false) -> Dma {
    return Dma{sQ, myreg + slot * SLOT, (32 * ((i + 4 * rot) & 15) + 16 * qh) * 8192 + (8 * hg + head(u)) * 256, 4 * 8192 - 1024, dq, nt, false};
  };
  auto mkWo = [&](int u, int dblk, int nq, int slot) -> Dma {
    return Dma{sO, myreg + slot * SLOT, ((8 * hg + head(u)) * 128 + 32 * dblk + 16 * qh) * 1024 + ((nq + rot + hg) & 3) * 256, 4 * 1024 - 1024, dO, false, false};
  };
#pragma unroll
  for (int i = 0; i < 8; ++i) {
    const unsigned m0 = ldsb + role * REG + (1 + qh) * SLOT + i * 1024, so = (8 * wid + i) * 1024;
    const unsigned v = 16 * (lane ^ (8 * qh + i));
    asm volatile("s_mov_b32 m0, %0\n\ts_nop 0\n\tbuffer_load_dwordx4 %1, %2, %3 offen nt lds" ::"s"(m0), "v"(v), "s"(sX), "s"(so) : "m0");
  }
  dma_all(mkWq(0, role, 3, true));
  dma_all(mkWq(0, 4 + role, 0, true));
  const unsigned tbl = ldsb + (4 * hh + (r16 >> 2)) * 256 + 64 * (r16 >> 2) + 32 * (g & 1) + 8 * (lane & 3);
  bf16x8 fa[10];
  auto rdT = [&](int f, unsigned tb, unsigned off, int ct, int s) {
    const unsigned a = (tb ^ (64u * ct)) + (off + 4096 * s);
    fa[f] = cat(trd(a), trd(a + 2048));
  };
  auto nop = [&](int) {};
  unsigned xb = ldsb + XB + qh * 10240 + lane * 16;
  PIN(xb);

  if (role == 0) {
    waitbar<8>();
    bf16x8 xf[32];
    {
      const unsigned row = 32 * qh + r32;
      const unsigned xa = ldsb + (row >> 4) * REG + (1 + ((row >> 3) & 1)) * SLOT + (row & 7) * 1024;
#pragma unroll
      for (int f = 0; f < 32; ++f) {
        const unsigned c = (2 * ((f + 8 * rot) & 31) + hh) ^ (row & 15);
        xf[f] = __builtin_bit_cast(bf16x8, swap32(__builtin_bit_cast(u32x4, ldsr128(xa + 16 * c))));
      }
    }
    waitbar<4>();
    STAMP(1);
    f32x16 qacc[4];
    unsigned tbh = tbl + 2 * REG;
    PIN(tbh);
    auto mmQ = [&](int i, int m) { qacc[m >> 1] = mfma(fa[m], xf[2 * i + (m & 1)], (i == 0 && (m & 1) == 0) ? zero16() : qacc[m >> 1]); };
#pragma unroll
    for (int j = 0; j < 4; ++j) {
      if (j == 0) { dma_all(mkWq(0, 8, 1, true)); dma_all(mkWq(0, 12, 2)); }
      else dma_all(mkWq(1, j - 1, (j + 2) & 3));
#pragma unroll
      for (int q = 0; q < 4; ++q) {
        const int i = 4 * j + q;
        pstep<8, 3, 2>([&](int m) { rdT(m, q < 2 ? tbl : tbh, (q & 1) * REG + ((j + 3) & 3) * SLOT, m >> 1, m & 1); },
                       [&](int m) { if (i > 0) mmQ(i - 1, m); }, [&](int m) { mmQ(i, m); }, nop);
      }
      waitbar<8>();
    }
    auto putQ = [&](int k) {
      if (k < 2) ldsw128(xb + 1024 * k, swap32(packacc1(qacc[0], k)));
      if (k == 2 || k == 3) ldsw128(xb + 1024 * k, swap32(packacc1(qacc[1], k - 2)));
      if (k == 4 || k == 5) ldsw128(xb + 1024 * k, swap32(packacc1(qacc[2], k - 4)));
      if (k == 6 || k == 7) ldsw128(xb + 1024 * (k + 2), swap32(packacc1(qacc[3], k - 6)));
    };
    for (int u = 1; u < 8; ++u) {
#pragma unroll
      for (int i = 0; i < 16; ++i) {
        const int un = i < 13 ? u : u + 1;
        Dma d = mkWq(un, (i + 3) & 15, (i + 2) & 3);
        if (i >= 13) d.so = un < 8 ? d.so : OOR;
        pstep<8, 3, 2>([&](int m) { rdT(m, tbl, ((i + 3) & 3) * SLOT, m >> 1, m & 1); },
                       [&](int m) { mmQ((i + 15) & 15, m); }, [&](int m) { mmQ(i, m); },
                       [&](int k) { dma_slot<0, 2>(d, k); if (i == 0) putQ(k); });
        waitbar<8>();
      }
    }
    mmQ(15, 5); mmQ(15, 6); mmQ(15, 7);
#pragma unroll
    for (int k = 0; k < 8; ++k) putQ(k);
    waitbar<0>();
#pragma unroll
    for (int j = 0; j < 4; ++j) { dma_all(mkWo(7, 0, (j + 1) & 3, (j + 1) & 3)); waitbar<63>(); waitbar<63>(); waitbar<63>(); }
    for (int s = 124; s < 136; ++s) waitbar<0>();
    waitbar<0>(); waitbar<0>();
  } else if (role == 1) {
    const unsigned dK = g * 256 + 16 * (r16 ^ g);
    auto mkK = [&](int u, int i, int slot) -> Dma { return Dma{sK, myreg + slot * SLOT, head(u) * 131072 + i * 8192 + qh * 4096, 0, dK, true, true}; };
    waitbar<8>(); waitbar<4>();
    dma_all(mkWq(0, 9, 1, true)); dma_all(mkWq(0, 13, 2)); waitbar<8>();
    waitbar<4>();
    dma_all(mkK(0, 0, 0)); waitbar<4>();
    dma_all(mkK(0, 1, 1)); waitbar<8>();
    dma_all(mkK(0, 2, 2)); waitbar<8>();
    const unsigned kb = ldsb + REG + r32 * 256 + 16 * (hh ^ (r32 & 15));
    unsigned pb = ldsb + PB + qh * 3072 + lane * 16;
    PIN(pb);
    f32x16 sb[4];
    bf16x8 qf[8];
#pragma unroll
    for (int i = 0; i < 4; ++i) sb[i] = zero16();
#pragma unroll
    for (int i = 0; i < 8; ++i) { qf[i] = (bf16x8){0, 0, 0, 0, 0, 0, 0, 0}; fa[i] = qf[i]; }
    float mrun = 0.f, lsum = 0.f, alpha = 0.f, nm = 0.f, ps = 0.f, mx = 0.f;
    auto mmK = [&](int i, int m) { sb[i & 3] = mfma(fa[m], qf[m], m == 0 ? zero16() : sb[i & 3]); };
    auto soft = [&](int i, int k, bool domax) {
      const int je = (i + 13) & 15, jm = (i + 15) & 15;
      f32x16& se = sb[je & 3];
      f32x16& sm = sb[jm & 3];
      if (k < 6) {
        const int e0 = k < 4 ? 3 * k : 12 + 2 * (k - 4), n = k < 4 ? 3 : 2;
#pragma unroll
        for (int e = e0; e < e0 + n; ++e) {
          se[e] = __builtin_amdgcn_exp2f(fmaf(se[e], SC, nm));
          ps = (e == 0 && (je & 1) == 0) ? se[e] : ps + se[e];
        }
        PIN(ps);
      }
      if (k == 3) ldsw128(pb + (je & 1) * 6144, packacc1(se, 0));
      if (k == 6) {
        ldsw128(pb + (je & 1) * 6144 + 1024, packacc1(se, 1));
        if ((je & 1) == 0) *(LAS float*)(size_t)(pb + 2048) = alpha;
        else {
          lsum = je == 1 ? ps : fmaf(lsum, alpha, ps);
          PIN(lsum);
          if (je == 15) *(LAS float*)(size_t)(pb + 6144 + 2052) = 1.0f / red_sum(lsum);
        }
      }
      if (domax) {
        if (k == 4) { mx = max3f((jm & 1) ? mx : (jm == 0 ? sm[0] : mrun), sm[0], sm[1]); mx = max3f(mx, sm[2], sm[3]); mx = max3f(mx, sm[4], sm[5]); }
        if (k == 5) { mx = max3f(mx, sm[6], sm[7]); mx = max3f(mx, sm[8], sm[9]); mx = max3f(mx, sm[10], sm[11]); }
        if (k == 6) { mx = max3f(mx, sm[12], sm[13]); mx = max3f(mx, sm[14], sm[15]); }
        if (k == 7 && (jm & 1)) {
          mx = red_max(mx);
          alpha = jm == 1 ? 0.f : __builtin_amdgcn_exp2f((mrun - mx) * SC);
          mrun = mx; nm = -mx * SC;
        }
      }
    };
    for (int u = 0; u < 8; ++u) {
      STAMP(2 + u);
#pragma unroll
      for (int i = 0; i < 16; ++i) {
        const int un = i < 13 ? u : u + 1;
        Dma d = mkK(un, (i + 3) & 15, (i + 3) & 3);
        if (i >= 13) d.so = un < 8 ? d.so : OOR;
        if (i == 0) {
#pragma unroll
          for (int f = 0; f < 5; ++f) qf[f] = ldsr128(xb + 1024 * f);
        }
        pstep<8, 3, 2>([&](int m) { fa[m] = ldsr128((kb ^ (32u * m)) + (i & 3) * SLOT); },
                       [&](int m) { mmK((i + 15) & 15, m); }, [&](int m) { mmK(i, m); },
                       [&](int k) {
                         dma_slot<1, 2>(d, k);
                         if (i == 0 && k == 3) { qf[5] = ldsr128(xb + 5120); qf[6] = ldsr128(xb + 8192); qf[7] = ldsr128(xb + 9216); }
                         soft(i, k, true);
                       });
        waitbar<8>();
      }
    }
    STAMP(10);
    mmK(15, 5); mmK(15, 6); mmK(15, 7);
    dma_all(mkWo(7, 1, 1, 1)); dma_all(mkWo(7, 1, 2, 2));
#pragma unroll
    for (int k = 0; k < 8; ++k) soft(0, k, true);
    waitbar<63>();
    dma_all(mkWo(7, 1, 3, 3)); dma_all(mkWo(7, 1, 0, 0));
#pragma unroll
    for (int k = 0; k < 8; ++k) soft(1, k, false);
    waitbar<63>();
#pragma unroll
    for (int k = 0; k < 8; ++k) soft(2, k, false);
    waitbar<63>();
    waitbar<63>();
    waitbar<0>();
    waitbar<0>(); waitbar<0>(); waitbar<0>();
    waitbar<0>(); waitbar<0>();
  } else {
    const rsrc_t sP = __builtin_amdgcn_make_buffer_rsrc((void*)ws, (short)0, WS_FLAG, 0x00020000);
    const unsigned grp = blockIdx.x & ~3u;
    unsigned tbh = tbl + 2 * REG;
    PIN(tbh);
    auto stP = [&](unsigned ln, const f32x16& a, int gi, int ct, int q) {
      const unsigned so = ((grp + ((hg + gi) & 3)) * 3 + gi - 1) * 32768 + qh * 16384 + ct * 4096;
      __builtin_amdgcn_raw_buffer_store_b128(__builtin_bit_cast(u32x4, (f32x4){a[4 * q], a[4 * q + 1], a[4 * q + 2], a[4 * q + 3]}), sP, ln * 16 + q * 1024, so, 16);
    };
    auto finish = [&](unsigned ln, const f32x16& a0, const u32x4* in, int ct) {
      f32x16 a = a0;
#pragma unroll
      for (int j = 0; j < 3; ++j)
#pragma unroll
        for (int q = 0; q < 4; ++q)
#pragma unroll
          for (int e = 0; e < 4; ++e) a[4 * q + e] += __uint_as_float(in[4 * j + q][e]);
      char* orow = out + ((size_t)b * 64 + 32 * qh + (ln & 31)) * 2048 + p * 1024 + ((rot + hg) & 3) * 256 + ct * 64 + (ln >> 5) * 16;
      *(u32x4*)(orow) = swap32(packacc1(a, 0));
      *(u32x4*)(orow + 32) = swap32(packacc1(a, 1));
    };
    auto ldP = [&](unsigned ln, u32x4* in, int ct) {
#pragma unroll
      for (int j = 0; j < 3; ++j)
#pragma unroll
        for (int q = 0; q < 4; ++q)
          in[4 * j + q] = __builtin_amdgcn_raw_buffer_load_b128(sP, ln * 16 + q * 1024, (blockIdx.x * 3 + j) * 32768 + qh * 16384 + ct * 4096, 16);
    };
    if (role == 2) {
      const unsigned dV = g * 256 + swT;
      auto mkV = [&](int u, int i, int slot) -> Dma { return Dma{sV, myreg + slot * SLOT, head(u) * 131072 + i * 8192 + qh * 4096, 0, dV, true, false}; };
#pragma unroll
      for (int i = 0; i < 10; ++i) ldsw128(xb + 1024 * i, (u32x4){0, 0, 0, 0});
      waitbar<8>(); waitbar<4>();
      dma_all(mkWq(0, 10, 1, true)); dma_all(mkWq(0, 14, 2)); waitbar<8>();
      waitbar<4>();
      waitbar<0>(); waitbar<0>(); waitbar<0>(); waitbar<0>();
      dma_all(mkV(0, 0, 0)); waitbar<8>();
      dma_all(mkV(0, 1, 1)); waitbar<8>();
      dma_all(mkV(0, 2, 2)); waitbar<8>();
      unsigned pb = ldsb + PB + qh * 3072 + lane * 16;
      PIN(pb);
      f32x16 o[4], oa[4];
      bf16x8 pf[2][2], of[2][2];
#pragma unroll
      for (int i = 0; i < 4; ++i) { o[i] = zero16(); oa[i] = zero16(); }
#pragma unroll
      for (int i = 0; i < 10; ++i) fa[i] = (bf16x8){0, 0, 0, 0, 0, 0, 0, 0};
      pf[0][0] = pf[0][1] = pf[1][0] = pf[1][1] = of[0][0] = of[0][1] = of[1][0] = of[1][1] = fa[0];
      float linv = 0.f;
      auto mm3 = [&](int j, int m) {
        if (m < 8) o[m >> 1] = mfma(fa[m], pf[j & 1][m & 1], (j == 0 && (m & 1) == 0) ? zero16() : o[m >> 1]);
        else { const int iw = (j + 15) & 15; oa[iw & 3] = mfma(fa[m], of[(iw >> 2) & 1][m & 1], oa[iw & 3]); }
      };
      auto putO = [&](int k) {
        if (k < 8) ldsw128(xb + 1024 * k, packacc(o[k >> 1], k & 1, linv));
      };
      for (int u = 0; u < 8; ++u) {
#pragma unroll
        for (int j = 0; j < 16; ++j) {
          const int un = j < 13 ? u : u + 1, iw = (j + 15) & 15;
          Dma d = mkV(un, (j + 3) & 15, (j + 3) & 3);
          if (j == 13) d.so = un < 8 ? d.so : OOR;
          const Dma de = mkWo(7, 2, (j - 13) & 3, (j + 3) & 3);
          pf[j & 1][0] = ldsr128(pb + (j & 1) * 6144); pf[j & 1][1] = ldsr128(pb + (j & 1) * 6144 + 1024);
          float alpha = 1.f;
          if ((j & 1) == 0 && j > 0) alpha = *(const LAS float*)(size_t)(pb + 2048);
          if ((iw & 3) == 0) { of[(iw >> 2) & 1][0] = ldsr128(xb + 2048 * (iw >> 2)); of[(iw >> 2) & 1][1] = ldsr128(xb + 2048 * (iw >> 2) + 1024); }
          pstep<10, 3, 2>([&](int m) { if (m < 8) rdT(m, tbh, (j & 3) * SLOT, m >> 1, m & 1); else rdT(m, tbh, REG + (j & 3) * SLOT, 3, m & 1); },
                          [&](int m) { mm3((j + 15) & 15, m); }, [&](int m) { mm3(j, m); },
                          [&](int k) {
                            if (j < 14) dma_slot<1, 2>(d, k);
                            else if (k == 1) { if (un < 8) dma_all(d); else dma_all(de); }
                            if (j == 0) putO(k);
                            if ((j & 1) == 0 && j > 0 && k >= 1 && k <= 8) {
                              const int ct = (k - 1) >> 1, h0 = 8 * ((k - 1) & 1);
#pragma unroll
                              for (int e = 0; e < 8; ++e) o[ct][h0 + e] *= alpha;
                            }
                            if (j == 15 && k == 9) linv = *(const LAS float*)(size_t)(pb + 6144 + 2052);
                          });
          waitbar<8>();
        }
      }
      dma_all(mkWo(7, 2, 3, 3));
      mm3(15, 7); mm3(15, 8); mm3(15, 9);
      rdT(8, tbh, REG, 3, 0); rdT(9, tbh, REG, 3, 1);
      bf16x8 og[8];
#pragma unroll
      for (int k = 0; k < 8; ++k) { const u32x4 w = packacc(o[k >> 1], k & 1, linv); ldsw128(xb + 1024 * k, w); og[k] = __builtin_bit_cast(bf16x8, w); }
      mm3(0, 8); mm3(0, 9);
      waitbar<8>();
      unsigned ln;
      asm volatile("v_mbcnt_lo_u32_b32 %0, -1, 0\n\tv_mbcnt_hi_u32_b32 %0, -1, %0" : "=v"(ln));
      const unsigned tl2 = tbh - 2 * REG;
#pragma unroll
      for (int j = 0; j < 4; ++j) {
        const int gi = (j + 1) & 3;
        if (j == 0) dma_all(mkWo(7, 2, 0, 0));
#pragma unroll
        for (int m = 0; m < 8; ++m) rdT(m, m < 4 ? tl2 : tbh, ((m >> 1) & 1) * REG + gi * SLOT, 3, m & 1);
#pragma unroll
        for (int m = 0; m < 8; ++m) oa[gi] = mfma(fa[m], og[m], oa[gi]);
        if (j < 3) {
#pragma unroll
          for (int q = 0; q < 4; ++q) stP(ln, oa[gi], gi, 3, q);
        }
        if (j == 0) waitbar<12>(); else if (j == 1) waitbar<12>(); else if (j == 2) waitbar<12>(); else waitbar<0>();
      }
      waitbar<0>();
      u32x4 in[12];
      ldP(ln, in, 3);
      finish(ln, oa[0], in, 3);
    } else {
      GAS unsigned long long* ep = (GAS unsigned long long*)(ws + WS_EPOCH + (blockIdx.x >> 2) * 128);
      unsigned long long tag;
      asm volatile("global_load_dwordx2 %0, %1, off sc1" : "=&v"(tag) : "v"(ep) : "memory");
      u32x4 wm[16];
#pragma unroll
      for (int i = 0; i < 16; ++i) {
        const unsigned so = ((8 * hg + head(0)) * 128 + 64 * qh + 4 * i) * 1024 + ((rot + hg) & 3) * 256;
        asm volatile("buffer_load_dwordx4 %0, %1, %2, %3 offen" : "=&v"(wm[i]) : "v"(dO), "s"(sO), "s"(so) : "memory");
      }
      waitbar<25>(); waitbar<21>();
      dma_all(mkWq(0, 11, 1, true)); dma_all(mkWq(0, 15, 2)); waitbar<25>();
      asm volatile("s_waitcnt vmcnt(4) lgkmcnt(0)\n\ts_barrier"
                   : "+v"(tag), "+v"(wm[0]), "+v"(wm[1]), "+v"(wm[2]), "+v"(wm[3]), "+v"(wm[4]), "+v"(wm[5]), "+v"(wm[6]), "+v"(wm[7]), "+v"(wm[8]), "+v"(wm[9]),
                     "+v"(wm[10]), "+v"(wm[11]), "+v"(wm[12]), "+v"(wm[13]), "+v"(wm[14]), "+v"(wm[15])::"memory");
      tag = ((unsigned long long)rfl((unsigned)(tag >> 32)) << 32) | rfl((unsigned)tag);
      tag = (tag ^ (tag >> 31)) * 0x9E3779B97F4A7C15ull + 0x632BE59BD9B4E019ull;
      waitbar<0>(); waitbar<0>(); waitbar<0>(); waitbar<0>();
#pragma unroll
      for (int s = 1; s <= 4; ++s) { Dma z = mkWo(0, 0, 0, (s + 3) & 3); z.so = OOR; dma_all(z); waitbar<8>(); }
      for (int s = 5; s <= 17; ++s) waitbar<0>();
      dma_all(mkWo(0, 0, 0, 1)); waitbar<8>();
      dma_all(mkWo(0, 0, 1, 2)); waitbar<8>();
      dma_all(mkWo(0, 0, 2, 3)); waitbar<8>();
      f32x16 oa[4][3];
      bf16x8 of[2][2];
#pragma unroll
      for (int i = 0; i < 12; ++i) oa[i / 3][i % 3] = zero16();
#pragma unroll
      for (int i = 0; i < 6; ++i) fa[i] = (bf16x8){0, 0, 0, 0, 0, 0, 0, 0};
      of[0][0] = of[0][1] = of[1][0] = of[1][1] = fa[0];
      auto mm4 = [&](int nq, int par, int m) { oa[nq][m >> 1] = mfma(fa[m], of[par][m & 1], oa[nq][m >> 1]); };
      for (int u = 0; u < 7; ++u) {
#pragma unroll
        for (int i = 0; i < 16; ++i) {
          const int un = i < 13 ? u : u + 1, in_ = (i + 3) & 15, ip = (i + 15) & 15;
          Dma d = mkWo(un, in_ >> 2, in_ & 3, i & 3);
          if (i >= 13) d.so = un < 7 ? d.so : mkWo(7, 3, (i - 12) & 3, 0).so;
          if ((i & 3) == 0) { of[(i >> 2) & 1][0] = ldsr128(xb + 2048 * (i >> 2)); of[(i >> 2) & 1][1] = ldsr128(xb + 2048 * (i >> 2) + 1024); }
          pstep<6, 2, 2>([&](int m) { rdT(m, tbh, REG + ((i + 1) & 3) * SLOT, m >> 1, m & 1); },
                         [&](int m) { mm4(ip & 3, (ip >> 2) & 1, m); }, [&](int m) { mm4(i & 3, (i >> 2) & 1, m); },
                         [&](int k) { dma_slot<0, 1>(d, k); });
          waitbar<8>();
        }
      }
      STAMP(11);
      unsigned ln;
      asm volatile("v_mbcnt_lo_u32_b32 %0, -1, 0\n\tv_mbcnt_hi_u32_b32 %0, -1, %0" : "=v"(ln));
      const unsigned tl2 = tbh - 2 * REG;
#pragma unroll
      for (int j = 0; j < 4; ++j) {
        const int gi = (j + 1) & 3;
        if (j == 0) dma_all(mkWo(7, 3, 0, 0));
#pragma unroll
        for (int q = 0; q < 4; ++q) {
          of[q & 1][0] = ldsr128(xb + 2048 * q); of[q & 1][1] = ldsr128(xb + 2048 * q + 1024);
          pstep<6, 2, 2>([&](int m) { rdT(m, q < 2 ? tl2 : tbh, (q & 1) * REG + gi * SLOT, m >> 1, m & 1); },
                         [&](int m) { if (q > 0) mm4(gi, (q - 1) & 1, m); else mm4(j == 0 ? 3 : j, 1, m); },
                         [&](int m) { mm4(gi, q & 1, m); },
                         [&](int k) {
                           if (j > 0 && q < 2) { const int n = 6 * q + k; stP(ln, oa[j][n >> 2], j, n >> 2, n & 3); }
                         });
          if (j == 3 && q == 1) {
            waitbar<0>();
            if (wid == 6 && ln < 3)
              __hip_atomic_store((GAS unsigned long long*)(ws + WS_FLAG + ((grp + ((hg + ln + 1) & 3)) * 4 + ln) * 128), tag, __ATOMIC_RELAXED, __HIP_MEMORY_SCOPE_AGENT);
          }
        }
        if (j == 0) waitbar<8>(); else if (j == 1) waitbar<16>(); else if (j == 2) waitbar<24>();
      }
      mm4(0, 1, 4); mm4(0, 1, 5);
      STAMP(12);
      if (wid == 6) {
        GAS unsigned long long* fl = (GAS unsigned long long*)(ws + WS_FLAG + (blockIdx.x * 4 + (ln < 3 ? ln : 0)) * 128);
        for (unsigned spins = 0; spins < (1u << 22); ++spins) {
          const unsigned long long v = __hip_atomic_load(fl, __ATOMIC_RELAXED, __HIP_MEMORY_SCOPE_AGENT);
          if (__all(v == tag)) break;
          __builtin_amdgcn_s_sleep(1);
        }
        if (hg == 0 && ln == 0) __hip_atomic_store(ep, tag, __ATOMIC_RELAXED, __HIP_MEMORY_SCOPE_AGENT);
      }
      waitbar<0>();
      STAMP(13);
      u32x4 in[3][12];
#pragma unroll
      for (int ct = 0; ct < 3; ++ct) ldP(ln, in[ct], ct);
#pragma unroll
      for (int ct = 0; ct < 3; ++ct) finish(ln, oa[0][ct], in[ct], ct);
      STAMP(14);
    }
  }
}

extern "C" void kernel_launch(void* const* d_in, const int* in_sizes, int n_in, void* d_out, int out_size, void* d_ws, size_t ws_size,
                              hipStream_t stream) {
  (void)in_sizes; (void)n_in; (void)out_size; (void)ws_size;
  (void)hipFuncSetAttribute((const void*)mha_fused, hipFuncAttributeMaxDynamicSharedMemorySize, LDS_BYTES);
  mha_fused<<<256, 512, LDS_BYTES, stream>>>((const char*)d_in[0], (const char*)d_in[1], (const char*)d_in[2], (const char*)d_in[3],
                                             (const char*)d_in[4], (const char*)d_in[5], (const char*)d_in[6], (const char*)d_in[7],
                                             (const char*)d_in[8], (const char*)d_in[9], (char*)d_ws, (char*)d_out);
}
```

```cpp
#include <hip/hip_runtime.h>
#include <hip/hip_bf16.h>

typedef __amdgpu_buffer_rsrc_t rsrc_t;
using bf16x8 = __attribute__((ext_vector_type(8))) short;
using s16x4 = __attribute__((ext_vector_type(4))) short;
using f32x4 = __attribute__((ext_vector_type(4))) float;
using f32x16 = __attribute__((ext_vector_type(16))) float;
using f32x2 = __attribute__((ext_vector_type(2))) float;
using u32x4 = __attribute__((ext_vector_type(4))) unsigned;
using bf16x2_t = __attribute__((ext_vector_type(2))) __bf16;
typedef short v4i16_t __attribute__((ext_vector_type(4)));
#define LAS __attribute__((address_space(3)))
#define GAS __attribute__((address_space(1)))
#define DI __device__ __forceinline__
#define SBAR() __builtin_amdgcn_sched_barrier(0)
#define PIN(x) asm volatile("" : "+v"(x))

constexpr int SLOT = 8192, REG = 32768;
constexpr int XB = 4 * REG;
constexpr int PB = XB + 2 * 10240;
constexpr int LDS_BYTES = PB + 4 * 3072;
constexpr unsigned WS_FLAG = 32u << 20, WS_EPOCH = 33u << 20;
constexpr float SC = 0.08838834764831845f * 1.4426950408889634f;
constexpr unsigned OOR = 0x80000000u;
constexpr unsigned NOLDS = 0x30000u;
#define THR 24.0f
#define D1 3
#define A1 2
#define D2 3
#define A2 2
#define D3 3
#define A3 2
#define D4 2
#define A4 2
#define NTM 1
#define ORD 1
#define R4K0 2
#define M0ONCE 1
#define ABL_KV 1
#define ABL_W 1
#define PRIO 0
#define WO_WARM 1

DI unsigned rfl(unsigned x) { return __builtin_amdgcn_readfirstlane(x); }
DI unsigned cvtpk(float lo, float hi) { f32x2 v = {lo, hi}; return __builtin_bit_cast(unsigned, __builtin_convertvector(v, bf16x2_t)); }
DI f32x16 mfma(bf16x8 a, bf16x8 b, f32x16 c) { return __builtin_amdgcn_mfma_f32_32x32x16_bf16(a, b, c, 0, 0, 0); }
DI s16x4 trd(unsigned addr) { return __builtin_bit_cast(s16x4, __builtin_amdgcn_ds_read_tr16_b64_v4i16((LAS v4i16_t*)(size_t)addr)); }
DI bf16x8 ldsr128(unsigned addr) { return *(const LAS bf16x8*)(size_t)addr; }
DI void ldsw128(unsigned addr, u32x4 v) { *(LAS u32x4*)(size_t)addr = v; }
DI bf16x8 cat(s16x4 a, s16x4 b) { return (bf16x8){a[0], a[1], a[2], a[3], b[0], b[1], b[2], b[3]}; }
DI f32x16 zero16() { return (f32x16){0.f, 0.f, 0.f, 0.f, 0.f, 0.f, 0.f, 0.f, 0.f, 0.f, 0.f, 0.f, 0.f, 0.f, 0.f, 0.f}; }
DI u32x4 packacc(const f32x16& a, int s, float f) {
  return (u32x4){cvtpk(a[8 * s] * f, a[8 * s + 1] * f), cvtpk(a[8 * s + 2] * f, a[8 * s + 3] * f), cvtpk(a[8 * s + 4] * f, a[8 * s + 5] * f), cvtpk(a[8 * s + 6] * f, a[8 * s + 7] * f)};
}
DI u32x4 packacc1(const f32x16& a, int s) {
  return (u32x4){cvtpk(a[8 * s], a[8 * s + 1]), cvtpk(a[8 * s + 2], a[8 * s + 3]), cvtpk(a[8 * s + 4], a[8 * s + 5]), cvtpk(a[8 * s + 6], a[8 * s + 7])};
}
DI u32x4 swap32(u32x4 w) {
  auto s0 = __builtin_amdgcn_permlane32_swap(w[0], w[2], false, false);
  auto s1 = __builtin_amdgcn_permlane32_swap(w[1], w[3], false, false);
  return (u32x4){s0[0], s1[0], s0[1], s1[1]};
}
DI float max3f(float a, float b, float c) { return __builtin_fmaxf(__builtin_fmaxf(a, b), c); }
DI float red_max(float v) {
  auto q = __builtin_amdgcn_permlane32_swap(__float_as_uint(v), __float_as_uint(v), false, false);
  return __builtin_fmaxf(__uint_as_float(q[0]), __uint_as_float(q[1]));
}
DI float red_sum(float v) {
  auto q = __builtin_amdgcn_permlane32_swap(__float_as_uint(v), __float_as_uint(v), false, false);
  return __uint_as_float(q[0]) + __uint_as_float(q[1]);
}
template <int N> DI void waitbar() { asm volatile("s_waitcnt vmcnt(%0) lgkmcnt(0)\n\ts_barrier" ::"i"(N) : "memory"); SBAR(); }
DI void bar() { asm volatile("s_waitcnt lgkmcnt(0)\n\ts_barrier" ::: "memory"); SBAR(); }

struct Dma { rsrc_t srd; unsigned m0, so, sstep, v0; bool nt, k; };
template <int J> DI void dma_piece(const Dma& d) {
  const unsigned v = d.k ? (d.v0 ^ (64u * J)) : d.v0;
  const unsigned so = d.so + J * d.sstep;
  if (J == 0 || !M0ONCE) {
    if (d.nt) asm volatile("s_mov_b32 m0, %0\n\ts_nop 0\n\tbuffer_load_dwordx4 %1, %2, %3 offen offset:%4 nt lds" ::"s"(d.m0), "v"(v), "s"(d.srd), "s"(so), "i"(1024 * J) : "m0");
    else asm volatile("s_mov_b32 m0, %0\n\ts_nop 0\n\tbuffer_load_dwordx4 %1, %2, %3 offen offset:%4 lds" ::"s"(d.m0), "v"(v), "s"(d.srd), "s"(so), "i"(1024 * J) : "m0");
  } else {
    if (d.nt) asm volatile("buffer_load_dwordx4 %0, %1, %2 offen offset:%3 nt lds" ::"v"(v), "s"(d.srd), "s"(so), "i"(1024 * J));
    else asm volatile("buffer_load_dwordx4 %0, %1, %2 offen offset:%3 lds" ::"v"(v), "s"(d.srd), "s"(so), "i"(1024 * J));
  }
}
DI void dma_all(const Dma& d) { dma_piece<0>(d); dma_piece<1>(d); dma_piece<2>(d); dma_piece<3>(d); }
template <int K0, int KS> DI void dma_slot(const Dma& d, int k) {
  if (k == K0) dma_piece<0>(d);
  if (k == K0 + KS) dma_piece<1>(d);
  if (k == K0 + 2 * KS) dma_piece<2>(d);
  if (k == K0 + 3 * KS) dma_piece<3>(d);
}
template <int N, int PH> DI void dma_pos(const Dma& d, int k, bool early) {
  if (ORD == 0) { if (N >= 8) dma_slot<PH, 2>(d, k); else dma_slot<0, 1>(d, k); }
  else if (early) dma_slot<0, 1>(d, k);
  else dma_slot<N - 4, 1>(d, k);
}
DI void dma_n(const Dma& a, const Dma& b, int n) {
  if (n == 0) dma_piece<0>(a); if (n == 1) dma_piece<1>(a); if (n == 2) dma_piece<2>(a); if (n == 3) dma_piece<3>(a);
  if (n == 4) dma_piece<0>(b); if (n == 5) dma_piece<1>(b); if (n == 6) dma_piece<2>(b); if (n == 7) dma_piece<3>(b);
}

template <int N, int D, int A, class RD, class MP, class MC, class FL>
DI void pstep(RD rd, MP mprev, MC mcur, FL fill) {
#pragma unroll
  for (int k = 0; k < A; ++k) rd(k);
  SBAR();
#pragma unroll
  for (int k = 0; k < N; ++k) {
    if (k < D) mprev(N - D + k); else mcur(k - D);
    SBAR();
    if (k + A < N) rd(k + A);
    fill(k);
    SBAR();
  }
}

#define STAMP(i)

__global__ __launch_bounds__(512, 2) void mha_fused(const char* __restrict__ xA, const char* __restrict__ kA, const char* __restrict__ vA,
                                                    const char* __restrict__ wqA, const char* __restrict__ woA, const char* __restrict__ xB,
                                                    const char* __restrict__ kB, const char* __restrict__ vB, const char* __restrict__ wqB,
                                                    const char* __restrict__ woB, char* __restrict__ ws, char* __restrict__ out) {
  extern __shared__ __attribute__((aligned(16))) char lds[];
  const unsigned tid = threadIdx.x, lane = tid & 63, wid = rfl(tid >> 6), role = wid >> 1, qh = wid & 1;
  const unsigned xg = blockIdx.x & 7, p = xg >> 2, hg = xg & 3, b = blockIdx.x >> 3;
  const unsigned rot = b >> 3;
  const char* kc = (p ? kB : kA) + (size_t)(b * 32 + hg * 8) * 131072;
  const char* vc = (p ? vB : vA) + (size_t)(b * 32 + hg * 8) * 131072;
  const char* wq = p ? wqB : wqA;
  const char* wo = p ? woB : woA;
  const char* xp = (p ? xB : xA) + (size_t)b * 65536;
  const rsrc_t sK = __builtin_amdgcn_make_buffer_rsrc((void*)kc, (short)0, ABL_KV << 20, 0x00020000);
  const rsrc_t sV = __builtin_amdgcn_make_buffer_rsrc((void*)vc, (short)0, ABL_KV << 20, 0x00020000);
  const rsrc_t sQ = __builtin_amdgcn_make_buffer_rsrc((void*)wq, (short)0, ABL_W << 22, 0x00020000);
  const rsrc_t sO = __builtin_amdgcn_make_buffer_rsrc((void*)wo, (short)0, ABL_W << 22, 0x00020000);
  const rsrc_t sX = __builtin_amdgcn_make_buffer_rsrc((void*)xp, (short)0, 65536, 0x00020000);
  const unsigned ldsb = (unsigned)(size_t)lds;
  const unsigned g = lane >> 4, r16 = lane & 15, r32 = lane & 31, hh = lane >> 5;
  STAMP(0);
#pragma unroll
  for (int i = 0; i < 8; ++i) {
    const unsigned m0 = ldsb + role * REG + (1 + qh) * SLOT + i * 1024, so = (8 * wid + i) * 1024;
    const unsigned v = 16 * (lane ^ (8 * qh + i));
    if (NTM & 8) asm volatile("s_mov_b32 m0, %0\n\ts_nop 0\n\tbuffer_load_dwordx4 %1, %2, %3 offen nt lds" ::"s"(m0), "v"(v), "s"(sX), "s"(so) : "m0");
    else asm volatile("s_mov_b32 m0, %0\n\ts_nop 0\n\tbuffer_load_dwordx4 %1, %2, %3 offen lds" ::"s"(m0), "v"(v), "s"(sX), "s"(so) : "m0");
  }
  const unsigned swT = 16 * (r16 ^ (4 * g));
  const unsigned dq = g * 8192 + swT, dO = g * 1024 + swT, dV = g * 256 + swT, dK = g * 256 + 16 * (r16 ^ g);
  const unsigned mq = ldsb + qh * 4096;
  auto head = [&](unsigned u) -> unsigned { return (u + b) & 7u; };
  auto mkWq = [&](unsigned u, unsigned i, unsigned reg, unsigned slot, bool nt = false) -> Dma {
    return Dma{sQ, mq + reg * REG + slot * SLOT, (32 * ((i + 4 * rot) & 15) + 16 * qh) * 8192 + (8 * hg + head(u)) * 256, 4 * 8192 - 1024, dq, nt, false};
  };
  auto mkWo = [&](unsigned u, unsigned dblk, unsigned nq, unsigned reg, unsigned slot) -> Dma {
    return Dma{sO, mq + reg * REG + slot * SLOT, ((8 * hg + head(u)) * 128 + 32 * dblk + 16 * qh) * 1024 + ((nq + rot + hg) & 3) * 256, 4 * 1024 - 1024, dO, false, false};
  };
  auto stK = [&](int sg) -> Dma {
    const bool ok = (unsigned)sg < 128u;
    return Dma{sK, ok ? mq + REG + (sg & 3) * SLOT : NOLDS, ok ? head(sg >> 4) * 131072 + (sg & 15) * 8192 + qh * 4096 : OOR, 0, dK, true, true};
  };
  auto stV = [&](int sg) -> Dma {
    const int t = sg - 3; const bool ok = (unsigned)t < 128u;
    return Dma{sV, ok ? mq + 2 * REG + (sg & 3) * SLOT : NOLDS, ok ? head(t >> 4) * 131072 + (t & 15) * 8192 + qh * 4096 : OOR, 0, dV, true, false};
  };
  auto stQ = [&](int sg) -> Dma {
    const int t = sg + 17; const bool ok = t < 128;
    Dma d = mkWq(t >> 4, t & 15, 0, sg & 3);
    d.m0 = ok ? d.m0 : NOLDS; d.so = ok ? d.so : OOR;
    return d;
  };
  auto stO = [&](int sg) -> Dma {
    const int t = sg - 20;
    Dma d = mkWo(t >> 4, (t >> 2) & 3, t & 3, 3, sg & 3);
    d.m0 = t < 112 ? d.m0 : NOLDS; d.so = (unsigned)t < 112u ? d.so : OOR;
    return d;
  };
  auto genA = [&](int sg, bool odd) -> Dma { return odd ? stQ(sg) : stK(sg); };
  auto genB = [&](int sg, bool odd) -> Dma { return odd ? stO(sg) : stV(sg); };
  asm volatile("s_barrier" ::: "memory");
  dma_all(mkWq(0, role, role, 3, NTM & 1));
  asm volatile("s_barrier" ::: "memory");
  dma_all(mkWq(0, 4 + role, role, 0, NTM & 2));
  const unsigned tbl = ldsb + (4 * hh + (r16 >> 2)) * 256 + 64 * (r16 >> 2) + 32 * (g & 1) + 8 * (lane & 3);
  unsigned tbh = tbl + 2 * REG;
  PIN(tbh);
  bf16x8 fa[10];
  auto rdT = [&](int f, unsigned tb, unsigned off, int ct, int s) {
    const unsigned a = (tb ^ (64u * ct)) + (off + 4096 * s);
    fa[f] = cat(trd(a), trd(a + 2048));
  };
  auto nop = [&](int) {};
  unsigned xb = ldsb + XB + qh * 10240 + lane * 16;
  PIN(xb);
  auto prologue = [&](bf16x8* xf, const int R, auto extra) {
    waitbar<8>();
    {
      const unsigned row = 32 * qh + r32;
      const unsigned xa = ldsb + (row >> 4) * REG + (1 + ((row >> 3) & 1)) * SLOT + (row & 7) * 1024;
#pragma unroll
      for (int f = 0; f < 32; ++f) {
        const unsigned c = (2 * ((f + 8 * rot) & 31) + hh) ^ (row & 15);
        xf[f] = __builtin_bit_cast(bf16x8, swap32(__builtin_bit_cast(u32x4, ldsr128(xa + 16 * c))));
      }
    }
    waitbar<4>();
    if (R == 0) STAMP(1);
    f32x16 q0 = zero16();
#pragma unroll
    for (int j = 0; j < 4; ++j) {
      if (j == 0) { dma_all(mkWq(0, 8 + R, R, 1, NTM & 4)); dma_all(mkWq(0, 12 + R, R, 2)); }
      if (j >= 1) { const int sg = j - 2; dma_all(R == 0 ? stQ(sg) : R == 1 ? stK(sg) : R == 2 ? stV(sg) : stO(sg)); }
      pstep<8, 3, 2>([&](int m) { rdT(m, m < 4 ? tbl : tbh, ((m >> 1) & 1) * REG + ((j + 3) & 3) * SLOT, R, m & 1); },
                     [&](int m) { if (j > 0) q0 = mfma(fa[m], xf[8 * (j - 1) + m], q0); },
                     [&](int m) { q0 = mfma(fa[m], xf[8 * j + m], q0); }, nop);
      if (j == 0) extra();
      constexpr int X = 17 * WO_WARM;
      if (j == 0) { if (R == 2) waitbar<8 + X>(); else waitbar<8>(); }
      if (j == 1) { if (R == 2) waitbar<8 + X>(); else waitbar<8>(); }
      if (j == 2) { if (R == 2) waitbar<8 + X>(); else waitbar<8>(); }
      if (j == 3) { if (R == 2) bar(); else waitbar<8>(); }
    }
    q0 = mfma(fa[5], xf[29], q0); q0 = mfma(fa[6], xf[30], q0); q0 = mfma(fa[7], xf[31], q0);
    ldsw128(xb + 2048 * R + (R == 3 ? 2048 : 0), swap32(packacc(q0, 0, SC)));
    ldsw128(xb + 2048 * R + (R == 3 ? 2048 : 0) + 1024, swap32(packacc(q0, 1, SC)));
    SBAR();
  };
  auto noextra = [&]() {};
  const rsrc_t sP = __builtin_amdgcn_make_buffer_rsrc((void*)ws, (short)0, WS_FLAG, 0x00020000);

  if (role == 0) {
    bf16x8 xf[32];
    prologue(xf, 0, noextra);
    f32x16 qacc[4];
#pragma unroll
    for (int i = 0; i < 4; ++i) qacc[i] = zero16();
    auto mmQ = [&](int i, int m) { qacc[m >> 1] = mfma(fa[m], xf[2 * i + (m & 1)], (i == 0 && (m & 1) == 0) ? zero16() : qacc[m >> 1]); };
    auto putQ = [&](int k) {
      ldsw128(xb + 1024 * (k < 6 ? k : k + 2), swap32(packacc(qacc[k >> 1], k & 1, SC)));
    };
    for (int u = 1; u < 8; ++u) {
#pragma unroll
      for (int i = 0; i < 16; ++i) {
        const Dma da = stQ(16 * (u - 1) + i + 2);
        pstep<8, D1, A1>([&](int m) { rdT(m, tbl, ((i + 3) & 3) * SLOT, m >> 1, m & 1); },
                       [&](int m) { mmQ((i + 15) & 15, m); }, [&](int m) { mmQ(i, m); },
                       [&](int k) { dma_pos<8, 0>(da, k, (i & 1) == 0); if (i == 0 && u > 1) putQ(k); });
        waitbar<8>();
      }
    }
#pragma unroll
    for (int m = 8 - D1; m < 8; ++m) mmQ(15, m);
#pragma unroll
    for (int k = 0; k < 8; ++k) putQ(k);
    bar();
#pragma unroll
    for (int j = 0; j < 4; ++j) { dma_all(mkWo(7, 0, j + 1, 0, j)); bar(); bar(); bar(); bar(); }
    dma_all(mkWo(7, 1, 1, 1, 0)); dma_all(mkWo(7, 1, 2, 1, 1)); bar();
    dma_all(mkWo(7, 2, 1, 2, 0)); dma_all(mkWo(7, 3, 1, 3, 0)); dma_all(mkWo(7, 1, 3, 1, 2)); bar();
    dma_all(mkWo(7, 2, 2, 2, 1)); dma_all(mkWo(7, 3, 2, 3, 1)); dma_all(mkWo(7, 1, 0, 1, 3)); bar();
    dma_all(mkWo(7, 2, 3, 2, 2)); dma_all(mkWo(7, 3, 3, 3, 2)); dma_all(mkWo(7, 2, 0, 2, 3)); waitbar<28>();
    dma_all(mkWo(7, 3, 0, 3, 3)); waitbar<20>();
    waitbar<8>();
    waitbar<0>();
    bar();
  } else if (role == 1) {
    { bf16x8 xf[32]; prologue(xf, 1, noextra); }
    dma_all(stK(2)); waitbar<8>();
    const unsigned kb = ldsb + REG + r32 * 256 + 16 * (hh ^ (r32 & 15));
    unsigned pb = ldsb + PB + qh * 3072 + lane * 16;
    PIN(pb);
    f32x16 sb[4], negm = zero16();
    bf16x8 qf[8];
#pragma unroll
    for (int i = 0; i < 4; ++i) sb[i] = zero16();
#pragma unroll
    for (int i = 0; i < 8; ++i) { qf[i] = (bf16x8){0, 0, 0, 0, 0, 0, 0, 0}; fa[i] = qf[i]; }
    float l0 = 0.f, l1 = 0.f, al = 1.f, mx = 0.f, my = 0.f;
    auto mmK = [&](int i, int m) { sb[i & 3] = mfma(fa[m], qf[m], m == 0 ? (i < 2 ? zero16() : negm) : sb[i & 3]); };
    auto soft = [&](int i, int k, bool chk) {
      const int je = (i + 14) & 15, jm = (i + 15) & 15;
      f32x16& se = sb[je & 3];
      f32x16& sm = sb[jm & 3];
      auto g0 = [](int q) { return q < 4 ? 3 * q : 12 + 2 * (q - 4); };
      auto gn = [](int q) { return q < 4 ? 3 : 2; };
      if (k < 6) {
#pragma unroll
        for (int e = g0(k); e < g0(k) + gn(k); ++e) se[e] = __builtin_amdgcn_exp2f(se[e]);
      }
      if (k >= 1 && k < 7) {
#pragma unroll
        for (int e = g0(k - 1); e < g0(k - 1) + gn(k - 1); ++e) {
          if (e & 1) l1 = (e == 1 && je == 0) ? se[e] : l1 + se[e];
          else l0 = (e == 0 && je == 0) ? se[e] : l0 + se[e];
        }
        PIN(l0); PIN(l1);
      }
      if (k == 0) *(LAS float*)(size_t)(pb + (je & 1) * 6144 + 2048) = al;
      if (k == 3) ldsw128(pb + (je & 1) * 6144, packacc1(se, 0));
      if (k == 6) {
        ldsw128(pb + (je & 1) * 6144 + 1024, packacc1(se, 1));
        if (je == 15) *(LAS float*)(size_t)(pb + 6144 + 2052) = 1.0f / red_sum(l0 + l1);
      }
      if (chk) {
        if (k == 4) { mx = max3f(sm[0], sm[1], sm[2]); my = max3f(sm[3], sm[4], sm[5]); mx = max3f(mx, sm[6], sm[7]); }
        if (k == 5) { my = max3f(my, sm[8], sm[9]); mx = max3f(mx, sm[10], sm[11]); my = max3f(my, sm[12], sm[13]); }
        if (k == 6) { mx = max3f(mx, sm[14], sm[15]); mx = __builtin_fmaxf(mx, my); }
        if (k == 7) {
          al = 1.f;
          if (jm == 0) {
            const float d = red_max(mx);
#pragma unroll
            for (int e = 0; e < 16; ++e) { sm[e] -= d; sb[i & 3][e] -= d; negm[e] = -d; }
          } else if (__builtin_expect(__builtin_amdgcn_ballot_w64(mx > THR) != 0, 0)) {
            const float d = __builtin_fmaxf(red_max(mx), 0.f);
            al = __builtin_amdgcn_exp2f(-d);
            l0 *= al; l1 *= al;
#pragma unroll
            for (int e = 0; e < 16; ++e) { sm[e] -= d; if (jm != 15) { sb[i & 3][e] -= d; negm[e] -= d; } }
          }
        }
      }
    };
    if (PRIO & 2) __builtin_amdgcn_s_setprio(1);
    for (int u = 0; u < 8; ++u) {
      STAMP(2 + u);
#pragma unroll
      for (int i = 0; i < 16; ++i) {
        const Dma db = stK(16 * u + i + 3);
        if (i == 0) {
#pragma unroll
          for (int f = 0; f < 8 - D2; ++f) qf[f] = ldsr128(xb + 1024 * f);
        }
        pstep<8, D2, A2>([&](int m) { fa[m] = ldsr128((kb ^ (32u * m)) + (i & 3) * SLOT); },
                       [&](int m) { mmK((i + 15) & 15, m); }, [&](int m) { mmK(i, m); },
                       [&](int k) {
                         if (i == 0 && k == D2) {
#pragma unroll
                           for (int f = 8 - D2; f < 8; ++f) qf[f] = ldsr128(xb + 1024 * (f < 6 ? f : f + 2));
                         }
                         dma_pos<8, 1>(db, k, (i & 1) == 0);
                         soft(i, k, true);
                       });
        waitbar<8>();
      }
    }
    STAMP(10);
#pragma unroll
    for (int m = 8 - D2; m < 8; ++m) mmK(15, m);
#pragma unroll
    for (int k = 0; k < 8; ++k) soft(0, k, true);
    bar();
#pragma unroll
    for (int k = 0; k < 8; ++k) soft(1, k, false);
    bar();
    bar(); bar(); bar(); bar(); bar();
    bar();
  } else {
    const unsigned grp = blockIdx.x & ~3u;
    auto stP = [&](unsigned ln, const f32x16& a, int gi, int ct, int q) {
      const unsigned so = ((grp + ((hg + gi) & 3)) * 3 + gi - 1) * 32768 + qh * 16384 + ct * 4096;
      __builtin_amdgcn_raw_buffer_store_b128(__builtin_bit_cast(u32x4, (f32x4){a[4 * q], a[4 * q + 1], a[4 * q + 2], a[4 * q + 3]}), sP, ln * 16 + q * 1024, so, 16);
    };
    auto putF = [&](unsigned ln, const f32x16& a, int ct) {
#pragma unroll
      for (int q = 0; q < 4; ++q)
        ldsw128(ldsb + ct * REG + qh * SLOT + q * 1024 + ln * 16, __builtin_bit_cast(u32x4, (f32x4){a[4 * q], a[4 * q + 1], a[4 * q + 2], a[4 * q + 3]}));
    };
    if (role == 2) {
#pragma unroll
      for (int i = 0; i < 10; ++i) ldsw128(xb + 1024 * i, (u32x4){0, 0, 0, 0});
      GAS unsigned long long* ep = (GAS unsigned long long*)(ws + WS_EPOCH + (blockIdx.x >> 2) * 128);
      unsigned long long tag;
      u32x4 wm[16];
      {
        bf16x8 xf[32];
        prologue(xf, 2, [&]() {
          if (WO_WARM) {
            asm volatile("global_load_dwordx2 %0, %1, off sc1" : "=&v"(tag) : "v"(ep) : "memory");
#pragma unroll
            for (int i = 0; i < 16; ++i) {
              const unsigned so = ((8 * hg + head(0)) * 128 + 64 * qh + 4 * i) * 1024 + ((rot + hg) & 3) * 256;
              asm volatile("buffer_load_dwordx4 %0, %1, %2, %3 offen" : "=&v"(wm[i]) : "v"(dO), "s"(sO), "s"(so) : "memory");
            }
          }
        });
      }
      if (!WO_WARM) {
        asm volatile("global_load_dwordx2 %0, %1, off sc1" : "=&v"(tag) : "v"(ep) : "memory");
#pragma unroll
        for (int i = 0; i < 16; ++i) wm[i] = (u32x4){0, 0, 0, 0};
      }
      bar();
      dma_all(stV(3)); bar();
      dma_all(stV(4)); bar();
      dma_all(stV(5));
      asm volatile("s_waitcnt vmcnt(8) lgkmcnt(0)\n\ts_barrier"
                   : "+v"(tag), "+v"(wm[0]), "+v"(wm[1]), "+v"(wm[2]), "+v"(wm[3]), "+v"(wm[4]), "+v"(wm[5]), "+v"(wm[6]), "+v"(wm[7]), "+v"(wm[8]), "+v"(wm[9]),
                     "+v"(wm[10]), "+v"(wm[11]), "+v"(wm[12]), "+v"(wm[13]), "+v"(wm[14]), "+v"(wm[15])::"memory");
      tag = ((unsigned long long)rfl((unsigned)(tag >> 32)) << 32) | rfl((unsigned)tag);
      tag = (tag ^ (tag >> 31)) * 0x9E3779B97F4A7C15ull + 0x632BE59BD9B4E019ull;
      unsigned pb = ldsb + PB + qh * 3072 + lane * 16;
      PIN(pb);
      f32x16 o[4], oa[4];
      bf16x8 pf[2][2], of[2][2];
#pragma unroll
      for (int i = 0; i < 4; ++i) { o[i] = zero16(); oa[i] = zero16(); }
#pragma unroll
      for (int i = 0; i < 10; ++i) fa[i] = (bf16x8){0, 0, 0, 0, 0, 0, 0, 0};
      pf[0][0] = pf[0][1] = pf[1][0] = pf[1][1] = of[0][0] = of[0][1] = of[1][0] = of[1][1] = fa[0];
      float linv = 0.f;
      auto mm3 = [&](int j, int m) {
        if (m < 8) o[m >> 1] = mfma(fa[m], pf[j & 1][m & 1], (j == 0 && (m & 1) == 0) ? zero16() : o[m >> 1]);
        else { const int iw = (j + 15) & 15; oa[iw & 3] = mfma(fa[m], of[(iw >> 2) & 1][m & 1], oa[iw & 3]); }
      };
      if (PRIO & 1) __builtin_amdgcn_s_setprio(1);
      for (int u = 0; u < 8; ++u) {
#pragma unroll
        for (int j = 0; j < 16; ++j) {
          const int iw = (j + 15) & 15;
          const Dma da = stV(16 * u + 6 + j);
          float alpha = 1.f;
          if (j > 0) alpha = *(const LAS float*)(size_t)(pb + (j & 1) * 6144 + 2048);
          pf[j & 1][0] = ldsr128(pb + (j & 1) * 6144); pf[j & 1][1] = ldsr128(pb + (j & 1) * 6144 + 1024);
          if ((iw & 3) == 0) { of[(iw >> 2) & 1][0] = ldsr128(xb + 2048 * (iw >> 2)); of[(iw >> 2) & 1][1] = ldsr128(xb + 2048 * (iw >> 2) + 1024); }
          pstep<10, D3, A3>([&](int m) { if (m < 8) rdT(m, tbh, ((j + 3) & 3) * SLOT, m >> 1, m & 1); else rdT(m, tbh, REG + ((j + 3) & 3) * SLOT, 3, m & 1); },
                          [&](int m) { mm3((j + 15) & 15, m); }, [&](int m) { mm3(j, m); },
                          [&](int k) {
                            dma_pos<10, 1>(da, k, (j & 1) == 1);
                            if (j == 0 && k < 8) ldsw128(xb + 1024 * k, packacc(o[k >> 1], k & 1, linv));
                            if (j > 0 && k == D3 - 1) {
                              if (__builtin_expect(__builtin_amdgcn_ballot_w64(alpha != 1.f) != 0, 0)) {
#pragma unroll
                                for (int ct = 0; ct < 4; ++ct) o[ct] *= alpha;
                              }
                            }
                            if (j == 15 && k == 9) linv = *(const LAS float*)(size_t)(pb + 6144 + 2052);
                          });
          waitbar<8>();
        }
      }
#pragma unroll
      for (int m = 10 - D3; m < 10; ++m) mm3(15, m);
      rdT(8, tbh, REG + 3 * SLOT, 3, 0); rdT(9, tbh, REG + 3 * SLOT, 3, 1);
      bf16x8 og[8];
#pragma unroll
      for (int k = 0; k < 8; ++k) { const u32x4 w = packacc(o[k >> 1], k & 1, linv); ldsw128(xb + 1024 * k, w); og[k] = __builtin_bit_cast(bf16x8, w); }
      mm3(0, 8); mm3(0, 9);
      bar();
      unsigned ln;
      asm volatile("v_mbcnt_lo_u32_b32 %0, -1, 0\n\tv_mbcnt_hi_u32_b32 %0, -1, %0" : "=v"(ln));
      const unsigned tl2 = tbh - 2 * REG;
#pragma unroll
      for (int j = 0; j < 4; ++j) {
        const int gi = (j + 1) & 3;
#pragma unroll
        for (int m = 0; m < 8; ++m) rdT(m, m < 4 ? tl2 : tbh, ((m >> 1) & 1) * REG + j * SLOT, 3, m & 1);
#pragma unroll
        for (int m = 0; m < 8; ++m) oa[gi] = mfma(fa[m], og[m], oa[gi]);
        if (j < 3) {
#pragma unroll
          for (int q = 0; q < 4; ++q) stP(ln, oa[gi], gi, 3, q);
          bar();
        }
      }
      putF(ln, oa[0], 3);
      waitbar<0>();
      if (wid == 4) {
        if (ln < 3)
          __hip_atomic_store((GAS unsigned long long*)(ws + WS_FLAG + ((grp + ((hg + ln + 1) & 3)) * 4 + ln) * 128), tag, __ATOMIC_RELAXED, __HIP_MEMORY_SCOPE_AGENT);
        GAS unsigned long long* fl = (GAS unsigned long long*)(ws + WS_FLAG + (blockIdx.x * 4 + (ln < 3 ? ln : 0)) * 128);
        for (unsigned spins = 0; spins < (1u << 22); ++spins) {
          const unsigned long long v = __hip_atomic_load(fl, __ATOMIC_RELAXED, __HIP_MEMORY_SCOPE_AGENT);
          if (__all(v == tag)) break;
          __builtin_amdgcn_s_sleep(1);
        }
        if (hg == 0 && ln == 0) __hip_atomic_store(ep, tag, __ATOMIC_RELAXED, __HIP_MEMORY_SCOPE_AGENT);
      }
    } else {
      { bf16x8 xf[32]; prologue(xf, 3, noextra); }
      dma_all(stO(2)); waitbar<8>();
      for (int s = 0; s < 17; ++s) waitbar<0>();
      dma_all(stO(20)); bar(); dma_all(stO(21)); bar(); dma_all(stO(22)); waitbar<8>();
      f32x16 oa[4][3];
      bf16x8 of[2][2];
#pragma unroll
      for (int i = 0; i < 12; ++i) oa[i / 3][i % 3] = zero16();
#pragma unroll
      for (int i = 0; i < 6; ++i) fa[i] = (bf16x8){0, 0, 0, 0, 0, 0, 0, 0};
      of[0][0] = of[0][1] = of[1][0] = of[1][1] = fa[0];
      auto mm4 = [&](int nq, int par, int m) { oa[nq][m >> 1] = mfma(fa[m], of[par][m & 1], oa[nq][m >> 1]); };
      for (int u = 0; u < 7; ++u) {
#pragma unroll
        for (int i = 0; i < 16; ++i) {
          const int ip = (i + 15) & 15, sg = 16 * u + 23 + i;
          const Dma db = stO(sg);
          if ((i & 3) == 0) { of[(i >> 2) & 1][0] = ldsr128(xb + 2048 * (i >> 2)); of[(i >> 2) & 1][1] = ldsr128(xb + 2048 * (i >> 2) + 1024); }
          pstep<6, D4, A4>([&](int m) { rdT(m, tbh, REG + (i & 3) * SLOT, m >> 1, m & 1); },
                         [&](int m) { mm4(ip & 3, (ip >> 2) & 1, m); }, [&](int m) { mm4(i & 3, (i >> 2) & 1, m); },
                         [&](int k) { dma_pos<6, 0>(db, k, (i & 1) == 1); });
          waitbar<8>();
        }
      }
      STAMP(11);
      unsigned ln;
      asm volatile("v_mbcnt_lo_u32_b32 %0, -1, 0\n\tv_mbcnt_hi_u32_b32 %0, -1, %0" : "=v"(ln));
      const unsigned tl2 = tbh - 2 * REG;
#pragma unroll
      for (int j = 0; j < 4; ++j) {
        const int gi = (j + 1) & 3;
#pragma unroll
        for (int q = 0; q < 4; ++q) {
          of[q & 1][0] = ldsr128(xb + 2048 * q); of[q & 1][1] = ldsr128(xb + 2048 * q + 1024);
          pstep<6, D4, A4>([&](int m) { rdT(m, q < 2 ? tl2 : tbh, (q & 1) * REG + j * SLOT, m >> 1, m & 1); },
                         [&](int m) { if (q > 0) mm4(gi, (q - 1) & 1, m); else mm4(j == 0 ? 3 : j, 1, m); },
                         [&](int m) { mm4(gi, q & 1, m); },
                         [&](int k) {
                           if (j > 0 && q < 2) { const int n = 6 * q + k; stP(ln, oa[j][n >> 2], j, n >> 2, n & 3); }
                         });
          if (j == 3 && q == 1) waitbar<0>();
        }
        if (j < 3) bar();
      }
#pragma unroll
      for (int m = 6 - D4; m < 6; ++m) mm4(0, 1, m);
#pragma unroll
      for (int ct = 0; ct < 3; ++ct) putF(ln, oa[0][ct], ct);
      STAMP(12);
    }
  }
  waitbar<0>();
  {
    unsigned ln;
    asm volatile("v_mbcnt_lo_u32_b32 %0, -1, 0\n\tv_mbcnt_hi_u32_b32 %0, -1, %0" : "=v"(ln));
    u32x4 in[12];
#pragma unroll
    for (int j = 0; j < 3; ++j)
#pragma unroll
      for (int q = 0; q < 4; ++q)
        in[4 * j + q] = __builtin_amdgcn_raw_buffer_load_b128(sP, ln * 16 + q * 1024, (blockIdx.x * 3 + j) * 32768 + qh * 16384 + role * 4096, 16);
    f32x16 a;
#pragma unroll
    for (int q = 0; q < 4; ++q) {
      const f32x4 own = __builtin_bit_cast(f32x4, ldsr128(ldsb + role * REG + qh * SLOT + q * 1024 + ln * 16));
#pragma unroll
      for (int e = 0; e < 4; ++e) a[4 * q + e] = ((own[e] + __uint_as_float(in[q][e])) + __uint_as_float(in[4 + q][e])) + __uint_as_float(in[8 + q][e]);
    }
    char* orow = out + ((size_t)b * 64 + 32 * qh + (ln & 31)) * 2048 + p * 1024 + ((rot + hg) & 3) * 256 + role * 64 + (ln >> 5) * 16;
    *(u32x4*)(orow) = swap32(packacc1(a, 0));
    *(u32x4*)(orow + 32) = swap32(packacc1(a, 1));
  }
}

extern "C" void kernel_launch(void* const* d_in, const int* in_sizes, int n_in, void* d_out, int out_size, void* d_ws, size_t ws_size,
                              hipStream_t stream) {
  (void)in_sizes; (void)n_in; (void)out_size; (void)ws_size;
  (void)hipFuncSetAttribute((const void*)mha_fused, hipFuncAttributeMaxDynamicSharedMemorySize, LDS_BYTES);
  mha_fused<<<256, 512, LDS_BYTES, stream>>>((const char*)d_in[0], (const char*)d_in[1], (const char*)d_in[2], (const char*)d_in[3],
                                             (const char*)d_in[4], (const char*)d_in[5], (const char*)d_in[6], (const char*)d_in[7],
                                             (const char*)d_in[8], (const char*)d_in[9], (char*)d_ws, (char*)d_out);
}
```

```cpp
#include <hip/hip_runtime.h>
#include <hip/hip_bf16.h>

typedef __amdgpu_buffer_rsrc_t rsrc_t;
using bf16x8 = __attribute__((ext_vector_type(8))) short;
using s16x4 = __attribute__((ext_vector_type(4))) short;
using f32x4 = __attribute__((ext_vector_type(4))) float;
using f32x16 = __attribute__((ext_vector_type(16))) float;
using f32x2 = __attribute__((ext_vector_type(2))) float;
using u32x4 = __attribute__((ext_vector_type(4))) unsigned;
using bf16x2_t = __attribute__((ext_vector_type(2))) __bf16;
typedef short v4i16_t __attribute__((ext_vector_type(4)));
#define LAS __attribute__((address_space(3)))
#define GAS __attribute__((address_space(1)))
#define DI __device__ __forceinline__
#define SBAR() __builtin_amdgcn_sched_barrier(0)
#define PIN(x) asm volatile("" : "+v"(x))

constexpr int SLOT = 8192, REG = 32768;
constexpr int XB = 4 * REG;
constexpr int PB = XB + 2 * 10240;
constexpr int LDS_BYTES = PB + 4 * 3072;
constexpr unsigned WS_FLAG = 32u << 20, WS_EPOCH = 33u << 20;
constexpr float SC = 0.08838834764831845f * 1.4426950408889634f;
constexpr unsigned OOR = 0x80000000u;
constexpr unsigned NOLDS = 0x30000u;
#define THR 24.0f
#define D1 3
#define A1 2
#define D2 3
#define A2 2
#define D3 3
#define A3 2
#define D4 2
#define A4 2
#define NTM 1
#define ORD 2
#define R4K0 2
#define M0ONCE 1
#define ABL_KV 1
#define ABL_W 1
#define PRIO 0
#define WO_WARM 1

DI unsigned rfl(unsigned x) { return __builtin_amdgcn_readfirstlane(x); }
DI unsigned cvtpk(float lo, float hi) { f32x2 v = {lo, hi}; return __builtin_bit_cast(unsigned, __builtin_convertvector(v, bf16x2_t)); }
DI f32x16 mfma(bf16x8 a, bf16x8 b, f32x16 c) { return __builtin_amdgcn_mfma_f32_32x32x16_bf16(a, b, c, 0, 0, 0); }
DI s16x4 trd(unsigned addr) { return __builtin_bit_cast(s16x4, __builtin_amdgcn_ds_read_tr16_b64_v4i16((LAS v4i16_t*)(size_t)addr)); }
DI bf16x8 ldsr128(unsigned addr) { return *(const LAS bf16x8*)(size_t)addr; }
DI void ldsw128(unsigned addr, u32x4 v) { *(LAS u32x4*)(size_t)addr = v; }
DI bf16x8 cat(s16x4 a, s16x4 b) { return (bf16x8){a[0], a[1], a[2], a[3], b[0], b[1], b[2], b[3]}; }
DI f32x16 zero16() { return (f32x16){0.f, 0.f, 0.f, 0.f, 0.f, 0.f, 0.f, 0.f, 0.f, 0.f, 0.f, 0.f, 0.f, 0.f, 0.f, 0.f}; }
DI u32x4 packacc(const f32x16& a, int s, float f) {
  return (u32x4){cvtpk(a[8 * s] * f, a[8 * s + 1] * f), cvtpk(a[8 * s + 2] * f, a[8 * s + 3] * f), cvtpk(a[8 * s + 4] * f, a[8 * s + 5] * f), cvtpk(a[8 * s + 6] * f, a[8 * s + 7] * f)};
}
DI u32x4 packacc1(const f32x16& a, int s) {
  return (u32x4){cvtpk(a[8 * s], a[8 * s + 1]), cvtpk(a[8 * s + 2], a[8 * s + 3]), cvtpk(a[8 * s + 4], a[8 * s + 5]), cvtpk(a[8 * s + 6], a[8 * s + 7])};
}
DI u32x4 swap32(u32x4 w) {
  auto s0 = __builtin_amdgcn_permlane32_swap(w[0], w[2], false, false);
  auto s1 = __builtin_amdgcn_permlane32_swap(w[1], w[3], false, false);
  return (u32x4){s0[0], s1[0], s0[1], s1[1]};
}
DI float max3f(float a, float b, float c) { return __builtin_fmaxf(__builtin_fmaxf(a, b), c); }
DI float red_max(float v) {
  auto q = __builtin_amdgcn_permlane32_swap(__float_as_uint(v), __float_as_uint(v), false, false);
  return __builtin_fmaxf(__uint_as_float(q[0]), __uint_as_float(q[1]));
}
DI float red_sum(float v) {
  auto q = __builtin_amdgcn_permlane32_swap(__float_as_uint(v), __float_as_uint(v), false, false);
  return __uint_as_float(q[0]) + __uint_as_float(q[1]);
}
template <int N> DI void waitbar() { asm volatile("s_waitcnt vmcnt(%0) lgkmcnt(0)\n\ts_barrier" ::"i"(N) : "memory"); SBAR(); }
DI void bar() { asm volatile("s_waitcnt lgkmcnt(0)\n\ts_barrier" ::: "memory"); SBAR(); }

struct Dma { rsrc_t srd; unsigned m0, so, sstep, v0; bool nt, k; };
template <int J> DI void dma_piece(const Dma& d) {
  const unsigned v = d.k ? (d.v0 ^ (64u * J)) : d.v0;
  const unsigned so = d.so + J * d.sstep;
  if (J == 0 || !M0ONCE) {
    if (d.nt) asm volatile("s_mov_b32 m0, %0\n\ts_nop 0\n\tbuffer_load_dwordx4 %1, %2, %3 offen offset:%4 nt lds" ::"s"(d.m0), "v"(v), "s"(d.srd), "s"(so), "i"(1024 * J) : "m0");
    else asm volatile("s_mov_b32 m0, %0\n\ts_nop 0\n\tbuffer_load_dwordx4 %1, %2, %3 offen offset:%4 lds" ::"s"(d.m0), "v"(v), "s"(d.srd), "s"(so), "i"(1024 * J) : "m0");
  } else {
    if (d.nt) asm volatile("buffer_load_dwordx4 %0, %1, %2 offen offset:%3 nt lds" ::"v"(v), "s"(d.srd), "s"(so), "i"(1024 * J));
    else asm volatile("buffer_load_dwordx4 %0, %1, %2 offen offset:%3 lds" ::"v"(v), "s"(d.srd), "s"(so), "i"(1024 * J));
  }
}
DI void dma_all(const Dma& d) { dma_piece<0>(d); dma_piece<1>(d); dma_piece<2>(d); dma_piece<3>(d); }
template <int K0, int KS> DI void dma_slot(const Dma& d, int k) {
  if (k == K0) dma_piece<0>(d);
  if (k == K0 + KS) dma_piece<1>(d);
  if (k == K0 + 2 * KS) dma_piece<2>(d);
  if (k == K0 + 3 * KS) dma_piece<3>(d);
}
template <int N, int PH> DI void dma_pos(const Dma& d, int k, bool early) {
  if (ORD == 0) { if (N >= 8) dma_slot<PH, 2>(d, k); else dma_slot<0, 1>(d, k); }
  else if (ORD == 1) { if (early) dma_slot<0, 1>(d, k); else dma_slot<N - 4, 1>(d, k); }
  else if (ORD == 2) { if (k == (early ? 0 : N - 1)) dma_all(d); }
  else if (ORD == 3) { const int k0 = early ? 0 : N - 2; if (k == k0) { dma_piece<0>(d); dma_piece<1>(d); } if (k == k0 + 1) { dma_piece<2>(d); dma_piece<3>(d); } }
  else if (ORD == 4) {
    if (PH == 1) { if (early) dma_slot<0, 1>(d, k); else dma_slot<N - 4, 1>(d, k); }
    else if (k == (early ? 0 : N - 1)) dma_all(d);
  }
}
DI void dma_n(const Dma& a, const Dma& b, int n) {
  if (n == 0) dma_piece<0>(a); if (n == 1) dma_piece<1>(a); if (n == 2) dma_piece<2>(a); if (n == 3) dma_piece<3>(a);
  if (n == 4) dma_piece<0>(b); if (n == 5) dma_piece<1>(b); if (n == 6) dma_piece<2>(b); if (n == 7) dma_piece<3>(b);
}

template <int N, int D, int A, class RD, class MP, class MC, class FL>
DI void pstep(RD rd, MP mprev, MC mcur, FL fill) {
#pragma unroll
  for (int k = 0; k < A; ++k) rd(k);
  SBAR();
#pragma unroll
  for (int k = 0; k < N; ++k) {
    if (k < D) mprev(N - D + k); else mcur(k - D);
    SBAR();
    if (k + A < N) rd(k + A);
    fill(k);
    SBAR();
  }
}

#define STAMP(i)

__global__ __launch_bounds__(512, 2) void mha_fused(const char* __restrict__ xA, const char* __restrict__ kA, const char* __restrict__ vA,
                                                    const char* __restrict__ wqA, const char* __restrict__ woA, const char* __restrict__ xB,
                                                    const char* __restrict__ kB, const char* __restrict__ vB, const char* __restrict__ wqB,
                                                    const char* __restrict__ woB, char* __restrict__ ws, char* __restrict__ out) {
  extern __shared__ __attribute__((aligned(16))) char lds[];
  const unsigned tid = threadIdx.x, lane = tid & 63, wid = rfl(tid >> 6), role = wid >> 1, qh = wid & 1;
  const unsigned xg = blockIdx.x & 7, p = xg >> 2, hg = xg & 3, b = blockIdx.x >> 3;
  const unsigned rot = b >> 3;
  const char* kc = (p ? kB : kA) + (size_t)(b * 32 + hg * 8) * 131072;
  const char* vc = (p ? vB : vA) + (size_t)(b * 32 + hg * 8) * 131072;
  const char* wq = p ? wqB : wqA;
  const char* wo = p ? woB : woA;
  const char* xp = (p ? xB : xA) + (size_t)b * 65536;
  const rsrc_t sK = __builtin_amdgcn_make_buffer_rsrc((void*)kc, (short)0, ABL_KV << 20, 0x00020000);
  const rsrc_t sV = __builtin_amdgcn_make_buffer_rsrc((void*)vc, (short)0, ABL_KV << 20, 0x00020000);
  const rsrc_t sQ = __builtin_amdgcn_make_buffer_rsrc((void*)wq, (short)0, ABL_W << 22, 0x00020000);
  const rsrc_t sO = __builtin_amdgcn_make_buffer_rsrc((void*)wo, (short)0, ABL_W << 22, 0x00020000);
  const rsrc_t sX = __builtin_amdgcn_make_buffer_rsrc((void*)xp, (short)0, 65536, 0x00020000);
  const unsigned ldsb = (unsigned)(size_t)lds;
  const unsigned g = lane >> 4, r16 = lane & 15, r32 = lane & 31, hh = lane >> 5;
  STAMP(0);
#pragma unroll
  for (int i = 0; i < 8; ++i) {
    const unsigned m0 = ldsb + role * REG + (1 + qh) * SLOT + i * 1024, so = (8 * wid + i) * 1024;
    const unsigned v = 16 * (lane ^ (8 * qh + i));
    if (NTM & 8) asm volatile("s_mov_b32 m0, %0\n\ts_nop 0\n\tbuffer_load_dwordx4 %1, %2, %3 offen nt lds" ::"s"(m0), "v"(v), "s"(sX), "s"(so) : "m0");
    else asm volatile("s_mov_b32 m0, %0\n\ts_nop 0\n\tbuffer_load_dwordx4 %1, %2, %3 offen lds" ::"s"(m0), "v"(v), "s"(sX), "s"(so) : "m0");
  }
  const unsigned swT = 16 * (r16 ^ (4 * g));
  const unsigned dq = g * 8192 + swT, dO = g * 1024 + swT, dV = g * 256 + swT, dK = g * 256 + 16 * (r16 ^ g);
  const unsigned mq = ldsb + qh * 4096;
  auto head = [&](unsigned u) -> unsigned { return (u + b) & 7u; };
  auto mkWq = [&](unsigned u, unsigned i, unsigned reg, unsigned slot, bool nt = false) -> Dma {
    return Dma{sQ, mq + reg * REG + slot * SLOT, (32 * ((i + 4 * rot) & 15) + 16 * qh) * 8192 + (8 * hg + head(u)) * 256, 4 * 8192 - 1024, dq, nt, false};
  };
  auto mkWo = [&](unsigned u, unsigned dblk, unsigned nq, unsigned reg, unsigned slot) -> Dma {
    return Dma{sO, mq + reg * REG + slot * SLOT, ((8 * hg + head(u)) * 128 + 32 * dblk + 16 * qh) * 1024 + ((nq + rot + hg) & 3) * 256, 4 * 1024 - 1024, dO, false, false};
  };
  auto stK = [&](int sg) -> Dma {
    const bool ok = (unsigned)sg < 128u;
    return Dma{sK, ok ? mq + REG + (sg & 3) * SLOT : NOLDS, ok ? head(sg >> 4) * 131072 + (sg & 15) * 8192 + qh * 4096 : OOR, 0, dK, true, true};
  };
  auto stV = [&](int sg) -> Dma {
    const int t = sg - 3; const bool ok = (unsigned)t < 128u;
    return Dma{sV, ok ? mq + 2 * REG + (sg & 3) * SLOT : NOLDS, ok ? head(t >> 4) * 131072 + (t & 15) * 8192 + qh * 4096 : OOR, 0, dV, true, false};
  };
  auto stQ = [&](int sg) -> Dma {
    const int t = sg + 17; const bool ok = t < 128;
    Dma d = mkWq(t >> 4, t & 15, 0, sg & 3);
    d.m0 = ok ? d.m0 : NOLDS; d.so = ok ? d.so : OOR;
    return d;
  };
  auto stO = [&](int sg) -> Dma {
    const int t = sg - 20;
    Dma d = mkWo(t >> 4, (t >> 2) & 3, t & 3, 3, sg & 3);
    d.m0 = t < 112 ? d.m0 : NOLDS; d.so = (unsigned)t < 112u ? d.so : OOR;
    return d;
  };
  auto genA = [&](int sg, bool odd) -> Dma { return odd ? stQ(sg) : stK(sg); };
  auto genB = [&](int sg, bool odd) -> Dma { return odd ? stO(sg) : stV(sg); };
  asm volatile("s_barrier" ::: "memory");
  dma_all(mkWq(0, role, role, 3, NTM & 1));
  asm volatile("s_barrier" ::: "memory");
  dma_all(mkWq(0, 4 + role, role, 0, NTM & 2));
  const unsigned tbl = ldsb + (4 * hh + (r16 >> 2)) * 256 + 64 * (r16 >> 2) + 32 * (g & 1) + 8 * (lane & 3);
  unsigned tbh = tbl + 2 * REG;
  PIN(tbh);
  bf16x8 fa[10];
  auto rdT = [&](int f, unsigned tb, unsigned off, int ct, int s) {
    const unsigned a = (tb ^ (64u * ct)) + (off + 4096 * s);
    fa[f] = cat(trd(a), trd(a + 2048));
  };
  auto nop = [&](int) {};
  unsigned xb = ldsb + XB + qh * 10240 + lane * 16;
  PIN(xb);
  auto prologue = [&](bf16x8* xf, const int R, auto extra) {
    waitbar<8>();
    {
      const unsigned row = 32 * qh + r32;
      const unsigned xa = ldsb + (row >> 4) * REG + (1 + ((row >> 3) & 1)) * SLOT + (row & 7) * 1024;
#pragma unroll
      for (int f = 0; f < 32; ++f) {
        const unsigned c = (2 * ((f + 8 * rot) & 31) + hh) ^ (row & 15);
        xf[f] = __builtin_bit_cast(bf16x8, swap32(__builtin_bit_cast(u32x4, ldsr128(xa + 16 * c))));
      }
    }
    waitbar<4>();
    if (R == 0) STAMP(1);
    f32x16 q0 = zero16();
#pragma unroll
    for (int j = 0; j < 4; ++j) {
      if (j == 0) { dma_all(mkWq(0, 8 + R, R, 1, NTM & 4)); dma_all(mkWq(0, 12 + R, R, 2)); }
      if (j >= 1) { const int sg = j - 2; dma_all(R == 0 ? stQ(sg) : R == 1 ? stK(sg) : R == 2 ? stV(sg) : stO(sg)); }
      pstep<8, 3, 2>([&](int m) { rdT(m, m < 4 ? tbl : tbh, ((m >> 1) & 1) * REG + ((j + 3) & 3) * SLOT, R, m & 1); },
                     [&](int m) { if (j > 0) q0 = mfma(fa[m], xf[8 * (j - 1) + m], q0); },
                     [&](int m) { q0 = mfma(fa[m], xf[8 * j + m], q0); }, nop);
      if (j == 0) extra();
      constexpr int X = 17 * WO_WARM;
      if (j == 0) { if (R == 2) waitbar<8 + X>(); else waitbar<8>(); }
      if (j == 1) { if (R == 2) waitbar<8 + X>(); else waitbar<8>(); }
      if (j == 2) { if (R == 2) waitbar<8 + X>(); else waitbar<8>(); }
      if (j == 3) { if (R == 2) bar(); else waitbar<8>(); }
    }
    q0 = mfma(fa[5], xf[29], q0); q0 = mfma(fa[6], xf[30], q0); q0 = mfma(fa[7], xf[31], q0);
    ldsw128(xb + 2048 * R + (R == 3 ? 2048 : 0), swap32(packacc(q0, 0, SC)));
    ldsw128(xb + 2048 * R + (R == 3 ? 2048 : 0) + 1024, swap32(packacc(q0, 1, SC)));
    SBAR();
  };
  auto noextra = [&]() {};
  const rsrc_t sP = __builtin_amdgcn_make_buffer_rsrc((void*)ws, (short)0, WS_FLAG, 0x00020000);

  if (role == 0) {
    bf16x8 xf[32];
    prologue(xf, 0, noextra);
    f32x16 qacc[4];
#pragma unroll
    for (int i = 0; i < 4; ++i) qacc[i] = zero16();
    auto mmQ = [&](int i, int m) { qacc[m >> 1] = mfma(fa[m], xf[2 * i + (m & 1)], (i == 0 && (m & 1) == 0) ? zero16() : qacc[m >> 1]); };
    auto putQ = [&](int k) {
      ldsw128(xb + 1024 * (k < 6 ? k : k + 2), swap32(packacc(qacc[k >> 1], k & 1, SC)));
    };
    for (int u = 1; u < 8; ++u) {
#pragma unroll
      for (int i = 0; i < 16; ++i) {
        const Dma da = stQ(16 * (u - 1) + i + 2);
        pstep<8, D1, A1>([&](int m) { rdT(m, tbl, ((i + 3) & 3) * SLOT, m >> 1, m & 1); },
                       [&](int m) { mmQ((i + 15) & 15, m); }, [&](int m) { mmQ(i, m); },
                       [&](int k) { dma_pos<8, 0>(da, k, (i & 1) == 0); if (i == 0 && u > 1) putQ(k); });
        waitbar<8>();
      }
    }
#pragma unroll
    for (int m = 8 - D1; m < 8; ++m) mmQ(15, m);
#pragma unroll
    for (int k = 0; k < 8; ++k) putQ(k);
    bar();
#pragma unroll
    for (int j = 0; j < 4; ++j) { dma_all(mkWo(7, 0, j + 1, 0, j)); bar(); bar(); bar(); bar(); }
    dma_all(mkWo(7, 1, 1, 1, 0)); dma_all(mkWo(7, 1, 2, 1, 1)); bar();
    dma_all(mkWo(7, 2, 1, 2, 0)); dma_all(mkWo(7, 3, 1, 3, 0)); dma_all(mkWo(7, 1, 3, 1, 2)); bar();
    dma_all(mkWo(7, 2, 2, 2, 1)); dma_all(mkWo(7, 3, 2, 3, 1)); dma_all(mkWo(7, 1, 0, 1, 3)); bar();
    dma_all(mkWo(7, 2, 3, 2, 2)); dma_all(mkWo(7, 3, 3, 3, 2)); dma_all(mkWo(7, 2, 0, 2, 3)); waitbar<28>();
    dma_all(mkWo(7, 3, 0, 3, 3)); waitbar<20>();
    waitbar<8>();
    waitbar<0>();
    bar();
  } else if (role == 1) {
    { bf16x8 xf[32]; prologue(xf, 1, noextra); }
    dma_all(stK(2)); waitbar<8>();
    const unsigned kb = ldsb + REG + r32 * 256 + 16 * (hh ^ (r32 & 15));
    unsigned pb = ldsb + PB + qh * 3072 + lane * 16;
    PIN(pb);
    f32x16 sb[4], negm = zero16();
    bf16x8 qf[8];
#pragma unroll
    for (int i = 0; i < 4; ++i) sb[i] = zero16();
#pragma unroll
    for (int i = 0; i < 8; ++i) { qf[i] = (bf16x8){0, 0, 0, 0, 0, 0, 0, 0}; fa[i] = qf[i]; }
    float l0 = 0.f, l1 = 0.f, al = 1.f, mx = 0.f, my = 0.f;
    auto mmK = [&](int i, int m) { sb[i & 3] = mfma(fa[m], qf[m], m == 0 ? (i < 2 ? zero16() : negm) : sb[i & 3]); };
    auto soft = [&](int i, int k, bool chk) {
      const int je = (i + 14) & 15, jm = (i + 15) & 15;
      f32x16& se = sb[je & 3];
      f32x16& sm = sb[jm & 3];
      auto g0 = [](int q) { return q < 4 ? 3 * q : 12 + 2 * (q - 4); };
      auto gn = [](int q) { return q < 4 ? 3 : 2; };
      if (k < 6) {
#pragma unroll
        for (int e = g0(k); e < g0(k) + gn(k); ++e) se[e] = __builtin_amdgcn_exp2f(se[e]);
      }
      if (k >= 1 && k < 7) {
#pragma unroll
        for (int e = g0(k - 1); e < g0(k - 1) + gn(k - 1); ++e) {
          if (e & 1) l1 = (e == 1 && je == 0) ? se[e] : l1 + se[e];
          else l0 = (e == 0 && je == 0) ? se[e] : l0 + se[e];
        }
        PIN(l0); PIN(l1);
      }
      if (k == 0) *(LAS float*)(size_t)(pb + (je & 1) * 6144 + 2048) = al;
      if (k == 3) ldsw128(pb + (je & 1) * 6144, packacc1(se, 0));
      if (k == 6) {
        ldsw128(pb + (je & 1) * 6144 + 1024, packacc1(se, 1));
        if (je == 15) *(LAS float*)(size_t)(pb + 6144 + 2052) = 1.0f / red_sum(l0 + l1);
      }
      if (chk) {
        if (k == 4) { mx = max3f(sm[0], sm[1], sm[2]); my = max3f(sm[3], sm[4], sm[5]); mx = max3f(mx, sm[6], sm[7]); }
        if (k == 5) { my = max3f(my, sm[8], sm[9]); mx = max3f(mx, sm[10], sm[11]); my = max3f(my, sm[12], sm[13]); }
        if (k == 6) { mx = max3f(mx, sm[14], sm[15]); mx = __builtin_fmaxf(mx, my); }
        if (k == 7) {
          al = 1.f;
          if (jm == 0) {
            const float d = red_max(mx);
#pragma unroll
            for (int e = 0; e < 16; ++e) { sm[e] -= d; sb[i & 3][e] -= d; negm[e] = -d; }
          } else if (__builtin_expect(__builtin_amdgcn_ballot_w64(mx > THR) != 0, 0)) {
            const float d = __builtin_fmaxf(red_max(mx), 0.f);
            al = __builtin_amdgcn_exp2f(-d);
            l0 *= al; l1 *= al;
#pragma unroll
            for (int e = 0; e < 16; ++e) { sm[e] -= d; if (jm != 15) { sb[i & 3][e] -= d; negm[e] -= d; } }
          }
        }
      }
    };
    if (PRIO & 2) __builtin_amdgcn_s_setprio(1);
    for (int u = 0; u < 8; ++u) {
      STAMP(2 + u);
#pragma unroll
      for (int i = 0; i < 16; ++i) {
        const Dma db = stK(16 * u + i + 3);
        if (i == 0) {
#pragma unroll
          for (int f = 0; f < 8 - D2; ++f) qf[f] = ldsr128(xb + 1024 * f);
        }
        pstep<8, D2, A2>([&](int m) { fa[m] = ldsr128((kb ^ (32u * m)) + (i & 3) * SLOT); },
                       [&](int m) { mmK((i + 15) & 15, m); }, [&](int m) { mmK(i, m); },
                       [&](int k) {
                         if (i == 0 && k == D2) {
#pragma unroll
                           for (int f = 8 - D2; f < 8; ++f) qf[f] = ldsr128(xb + 1024 * (f < 6 ? f : f + 2));
                         }
                         dma_pos<8, 1>(db, k, (i & 1) == 0);
                         soft(i, k, true);
                       });
        waitbar<8>();
      }
    }
    STAMP(10);
#pragma unroll
    for (int m = 8 - D2; m < 8; ++m) mmK(15, m);
#pragma unroll
    for (int k = 0; k < 8; ++k) soft(0, k, true);
    bar();
#pragma unroll
    for (int k = 0; k < 8; ++k) soft(1, k, false);
    bar();
    bar(); bar(); bar(); bar(); bar();
    bar();
  } else {
    const unsigned grp = blockIdx.x & ~3u;
    auto stP = [&](unsigned ln, const f32x16& a, int gi, int ct, int q) {
      const unsigned so = ((grp + ((hg + gi) & 3)) * 3 + gi - 1) * 32768 + qh * 16384 + ct * 4096;
      __builtin_amdgcn_raw_buffer_store_b128(__builtin_bit_cast(u32x4, (f32x4){a[4 * q], a[4 * q + 1], a[4 * q + 2], a[4 * q + 3]}), sP, ln * 16 + q * 1024, so, 16);
    };
    auto putF = [&](unsigned ln, const f32x16& a, int ct) {
#pragma unroll
      for (int q = 0; q < 4; ++q)
        ldsw128(ldsb + ct * REG + qh * SLOT + q * 1024 + ln * 16, __builtin_bit_cast(u32x4, (f32x4){a[4 * q], a[4 * q + 1], a[4 * q + 2], a[4 * q + 3]}));
    };
    if (role == 2) {
#pragma unroll
      for (int i = 0; i < 10; ++i) ldsw128(xb + 1024 * i, (u32x4){0, 0, 0, 0});
      GAS unsigned long long* ep = (GAS unsigned long long*)(ws + WS_EPOCH + (blockIdx.x >> 2) * 128);
      unsigned long long tag;
      u32x4 wm[16];
      {
        bf16x8 xf[32];
        prologue(xf, 2, [&]() {
          if (WO_WARM) {
            asm volatile("global_load_dwordx2 %0, %1, off sc1" : "=&v"(tag) : "v"(ep) : "memory");
#pragma unroll
            for (int i = 0; i < 16; ++i) {
              const unsigned so = ((8 * hg + head(0)) * 128 + 64 * qh + 4 * i) * 1024 + ((rot + hg) & 3) * 256;
              asm volatile("buffer_load_dwordx4 %0, %1, %2, %3 offen" : "=&v"(wm[i]) : "v"(dO), "s"(sO), "s"(so) : "memory");
            }
          }
        });
      }
      if (!WO_WARM) {
        asm volatile("global_load_dwordx2 %0, %1, off sc1" : "=&v"(tag) : "v"(ep) : "memory");
#pragma unroll
        for (int i = 0; i < 16; ++i) wm[i] = (u32x4){0, 0, 0, 0};
      }
      bar();
      dma_all(stV(3)); bar();
      dma_all(stV(4)); bar();
      dma_all(stV(5));
      asm volatile("s_waitcnt vmcnt(8) lgkmcnt(0)\n\ts_barrier"
                   : "+v"(tag), "+v"(wm[0]), "+v"(wm[1]), "+v"(wm[2]), "+v"(wm[3]), "+v"(wm[4]), "+v"(wm[5]), "+v"(wm[6]), "+v"(wm[7]), "+v"(wm[8]), "+v"(wm[9]),
                     "+v"(wm[10]), "+v"(wm[11]), "+v"(wm[12]), "+v"(wm[13]), "+v"(wm[14]), "+v"(wm[15])::"memory");
      tag = ((unsigned long long)rfl((unsigned)(tag >> 32)) << 32) | rfl((unsigned)tag);
      tag = (tag ^ (tag >> 31)) * 0x9E3779B97F4A7C15ull + 0x632BE59BD9B4E019ull;
      unsigned pb = ldsb + PB + qh * 3072 + lane * 16;
      PIN(pb);
      f32x16 o[4], oa[4];
      bf16x8 pf[2][2], of[2][2];
#pragma unroll
      for (int i = 0; i < 4; ++i) { o[i] = zero16(); oa[i] = zero16(); }
#pragma unroll
      for (int i = 0; i < 10; ++i) fa[i] = (bf16x8){0, 0, 0, 0, 0, 0, 0, 0};
      pf[0][0] = pf[0][1] = pf[1][0] = pf[1][1] = of[0][0] = of[0][1] = of[1][0] = of[1][1] = fa[0];
      float linv = 0.f;
      auto mm3 = [&](int j, int m) {
        if (m < 8) o[m >> 1] = mfma(fa[m], pf[j & 1][m & 1], (j == 0 && (m & 1) == 0) ? zero16() : o[m >> 1]);
        else { const int iw = (j + 15) & 15; oa[iw & 3] = mfma(fa[m], of[(iw >> 2) & 1][m & 1], oa[iw & 3]); }
      };
      if (PRIO & 1) __builtin_amdgcn_s_setprio(1);
      for (int u = 0; u < 8; ++u) {
#pragma unroll
        for (int j = 0; j < 16; ++j) {
          const int iw = (j + 15) & 15;
          const Dma da = stV(16 * u + 6 + j);
          float alpha = 1.f;
          if (j > 0) alpha = *(const LAS float*)(size_t)(pb + (j & 1) * 6144 + 2048);
          pf[j & 1][0] = ldsr128(pb + (j & 1) * 6144); pf[j & 1][1] = ldsr128(pb + (j & 1) * 6144 + 1024);
          if ((iw & 3) == 0) { of[(iw >> 2) & 1][0] = ldsr128(xb + 2048 * (iw >> 2)); of[(iw >> 2) & 1][1] = ldsr128(xb + 2048 * (iw >> 2) + 1024); }
          pstep<10, D3, A3>([&](int m) { if (m < 8) rdT(m, tbh, ((j + 3) & 3) * SLOT, m >> 1, m & 1); else rdT(m, tbh, REG + ((j + 3) & 3) * SLOT, 3, m & 1); },
                          [&](int m) { mm3((j + 15) & 15, m); }, [&](int m) { mm3(j, m); },
                          [&](int k) {
                            dma_pos<10, 1>(da, k, (j & 1) == 1);
                            if (j == 0 && k < 8) ldsw128(xb + 1024 * k, packacc(o[k >> 1], k & 1, linv));
                            if (j > 0 && k == D3 - 1) {
                              if (__builtin_expect(__builtin_amdgcn_ballot_w64(alpha != 1.f) != 0, 0)) {
#pragma unroll
                                for (int ct = 0; ct < 4; ++ct) o[ct] *= alpha;
                              }
                            }
                            if (j == 15 && k == 9) linv = *(const LAS float*)(size_t)(pb + 6144 + 2052);
                          });
          waitbar<8>();
        }
      }
#pragma unroll
      for (int m = 10 - D3; m < 10; ++m) mm3(15, m);
      rdT(8, tbh, REG + 3 * SLOT, 3, 0); rdT(9, tbh, REG + 3 * SLOT, 3, 1);
      bf16x8 og[8];
#pragma unroll
      for (int k = 0; k < 8; ++k) { const u32x4 w = packacc(o[k >> 1], k & 1, linv); ldsw128(xb + 1024 * k, w); og[k] = __builtin_bit_cast(bf16x8, w); }
      mm3(0, 8); mm3(0, 9);
      bar();
      unsigned ln;
      asm volatile("v_mbcnt_lo_u32_b32 %0, -1, 0\n\tv_mbcnt_hi_u32_b32 %0, -1, %0" : "=v"(ln));
      const unsigned tl2 = tbh - 2 * REG;
#pragma unroll
      for (int j = 0; j < 4; ++j) {
        const int gi = (j + 1) & 3;
#pragma unroll
        for (int m = 0; m < 8; ++m) rdT(m, m < 4 ? tl2 : tbh, ((m >> 1) & 1) * REG + j * SLOT, 3, m & 1);
#pragma unroll
        for (int m = 0; m < 8; ++m) oa[gi] = mfma(fa[m], og[m], oa[gi]);
        if (j < 3) {
#pragma unroll
          for (int q = 0; q < 4; ++q) stP(ln, oa[gi], gi, 3, q);
          bar();
        }
      }
      putF(ln, oa[0], 3);
      waitbar<0>();
      if (wid == 4) {
        if (ln < 3)
          __hip_atomic_store((GAS unsigned long long*)(ws + WS_FLAG + ((grp + ((hg + ln + 1) & 3)) * 4 + ln) * 128), tag, __ATOMIC_RELAXED, __HIP_MEMORY_SCOPE_AGENT);
        GAS unsigned long long* fl = (GAS unsigned long long*)(ws + WS_FLAG + (blockIdx.x * 4 + (ln < 3 ? ln : 0)) * 128);
        for (unsigned spins = 0; spins < (1u << 22); ++spins) {
          const unsigned long long v = __hip_atomic_load(fl, __ATOMIC_RELAXED, __HIP_MEMORY_SCOPE_AGENT);
          if (__all(v == tag)) break;
          __builtin_amdgcn_s_sleep(1);
        }
        if (hg == 0 && ln == 0) __hip_atomic_store(ep, tag, __ATOMIC_RELAXED, __HIP_MEMORY_SCOPE_AGENT);
      }
    } else {
      { bf16x8 xf[32]; prologue(xf, 3, noextra); }
      dma_all(stO(2)); waitbar<8>();
      for (int s = 0; s < 17; ++s) waitbar<0>();
      dma_all(stO(20)); bar(); dma_all(stO(21)); bar(); dma_all(stO(22)); waitbar<8>();
      f32x16 oa[4][3];
      bf16x8 of[2][2];
#pragma unroll
      for (int i = 0; i < 12; ++i) oa[i / 3][i % 3] = zero16();
#pragma unroll
      for (int i = 0; i < 6; ++i) fa[i] = (bf16x8){0, 0, 0, 0, 0, 0, 0, 0};
      of[0][0] = of[0][1] = of[1][0] = of[1][1] = fa[0];
      auto mm4 = [&](int nq, int par, int m) { oa[nq][m >> 1] = mfma(fa[m], of[par][m & 1], oa[nq][m >> 1]); };
      for (int u = 0; u < 7; ++u) {
#pragma unroll
        for (int i = 0; i < 16; ++i) {
          const int ip = (i + 15) & 15, sg = 16 * u + 23 + i;
          const Dma db = stO(sg);
          if ((i & 3) == 0) { of[(i >> 2) & 1][0] = ldsr128(xb + 2048 * (i >> 2)); of[(i >> 2) & 1][1] = ldsr128(xb + 2048 * (i >> 2) + 1024); }
          pstep<6, D4, A4>([&](int m) { rdT(m, tbh, REG + (i & 3) * SLOT, m >> 1, m & 1); },
                         [&](int m) { mm4(ip & 3, (ip >> 2) & 1, m); }, [&](int m) { mm4(i & 3, (i >> 2) & 1, m); },
                         [&](int k) { dma_pos<6, 0>(db, k, (i & 1) == 1); });
          waitbar<8>();
        }
      }
      STAMP(11);
      unsigned ln;
      asm volatile("v_mbcnt_lo_u32_b32 %0, -1, 0\n\tv_mbcnt_hi_u32_b32 %0, -1, %0" : "=v"(ln));
      const unsigned tl2 = tbh - 2 * REG;
#pragma unroll
      for (int j = 0; j < 4; ++j) {
        const int gi = (j + 1) & 3;
#pragma unroll
        for (int q = 0; q < 4; ++q) {
          of[q & 1][0] = ldsr128(xb + 2048 * q); of[q & 1][1] = ldsr128(xb + 2048 * q + 1024);
          pstep<6, D4, A4>([&](int m) { rdT(m, q < 2 ? tl2 : tbh, (q & 1) * REG + j * SLOT, m >> 1, m & 1); },
                         [&](int m) { if (q > 0) mm4(gi, (q - 1) & 1, m); else mm4(j == 0 ? 3 : j, 1, m); },
                         [&](int m) { mm4(gi, q & 1, m); },
                         [&](int k) {
                           if (j > 0 && q < 2) { const int n = 6 * q + k; stP(ln, oa[j][n >> 2], j, n >> 2, n & 3); }
                         });
          if (j == 3 && q == 1) waitbar<0>();
        }
        if (j < 3) bar();
      }
#pragma unroll
      for (int m = 6 - D4; m < 6; ++m) mm4(0, 1, m);
#pragma unroll
      for (int ct = 0; ct < 3; ++ct) putF(ln, oa[0][ct], ct);
      STAMP(12);
    }
  }
  waitbar<0>();
  {
    unsigned ln;
    asm volatile("v_mbcnt_lo_u32_b32 %0, -1, 0\n\tv_mbcnt_hi_u32_b32 %0, -1, %0" : "=v"(ln));
    u32x4 in[12];
#pragma unroll
    for (int j = 0; j < 3; ++j)
#pragma unroll
      for (int q = 0; q < 4; ++q)
        in[4 * j + q] = __builtin_amdgcn_raw_buffer_load_b128(sP, ln * 16 + q * 1024, (blockIdx.x * 3 + j) * 32768 + qh * 16384 + role * 4096, 16);
    f32x16 a;
#pragma unroll
    for (int q = 0; q < 4; ++q) {
      const f32x4 own = __builtin_bit_cast(f32x4, ldsr128(ldsb + role * REG + qh * SLOT + q * 1024 + ln * 16));
#pragma unroll
      for (int e = 0; e < 4; ++e) a[4 * q + e] = ((own[e] + __uint_as_float(in[q][e])) + __uint_as_float(in[4 + q][e])) + __uint_as_float(in[8 + q][e]);
    }
    char* orow = out + ((size_t)b * 64 + 32 * qh + (ln & 31)) * 2048 + p * 1024 + ((rot + hg) & 3) * 256 + role * 64 + (ln >> 5) * 16;
    *(u32x4*)(orow) = swap32(packacc1(a, 0));
    *(u32x4*)(orow + 32) = swap32(packacc1(a, 1));
  }
}

extern "C" void kernel_launch(void* const* d_in, const int* in_sizes, int n_in, void* d_out, int out_size, void* d_ws, size_t ws_size,
                              hipStream_t stream) {
  (void)in_sizes; (void)n_in; (void)out_size; (void)ws_size;
  (void)hipFuncSetAttribute((const void*)mha_fused, hipFuncAttributeMaxDynamicSharedMemorySize, LDS_BYTES);
  mha_fused<<<256, 512, LDS_BYTES, stream>>>((const char*)d_in[0], (const char*)d_in[1], (const char*)d_in[2], (const char*)d_in[3],
                                             (const char*)d_in[4], (const char*)d_in[5], (const char*)d_in[6], (const char*)d_in[7],
                                             (const char*)d_in[8], (const char*)d_in[9], (char*)d_ws, (char*)d_out);
}
```

```cpp
#include <hip/hip_runtime.h>
#include <hip/hip_bf16.h>

typedef __amdgpu_buffer_rsrc_t rsrc_t;
using bf16x8 = __attribute__((ext_vector_type(8))) short;
using s16x4 = __attribute__((ext_vector_type(4))) short;
using f32x4 = __attribute__((ext_vector_type(4))) float;
using f32x16 = __attribute__((ext_vector_type(16))) float;
using f32x2 = __attribute__((ext_vector_type(2))) float;
using u32x4 = __attribute__((ext_vector_type(4))) unsigned;
using bf16x2_t = __attribute__((ext_vector_type(2))) __bf16;
typedef short v4i16_t __attribute__((ext_vector_type(4)));
#define LAS __attribute__((address_space(3)))
#define GAS __attribute__((address_space(1)))
#define DI __device__ __forceinline__
#define SBAR() __builtin_amdgcn_sched_barrier(0)
#define PIN(x) asm volatile("" : "+v"(x))

constexpr int SLOT = 8192, REG = 32768;
constexpr int XB = 4 * REG;
constexpr int PB = XB + 2 * 10240;
constexpr int LDS_BYTES = PB + 4 * 3072;
constexpr unsigned WS_FLAG = 32u << 20, WS_EPOCH = 33u << 20;
constexpr float SC = 0.08838834764831845f * 1.4426950408889634f;
constexpr unsigned OOR = 0x80000000u;
constexpr unsigned NOLDS = 0x30000u;
#define THR 24.0f
#define D1 3
#define A1 2
#define D2 3
#define A2 2
#define D3 3
#define A3 2
#define D4 2
#define A4 2
#define NTM 1
#define ORD 1
#define ROLEMAP 0x3210
#define MFIRST 1
#define R4K0 2
#define M0ONCE 1
#define ABL_KV 1
#define ABL_W 1
#define PRIO 0
#define WO_WARM 1

DI unsigned rfl(unsigned x) { return __builtin_amdgcn_readfirstlane(x); }
DI unsigned cvtpk(float lo, float hi) { f32x2 v = {lo, hi}; return __builtin_bit_cast(unsigned, __builtin_convertvector(v, bf16x2_t)); }
DI f32x16 mfma(bf16x8 a, bf16x8 b, f32x16 c) { return __builtin_amdgcn_mfma_f32_32x32x16_bf16(a, b, c, 0, 0, 0); }
DI s16x4 trd(unsigned addr) { return __builtin_bit_cast(s16x4, __builtin_amdgcn_ds_read_tr16_b64_v4i16((LAS v4i16_t*)(size_t)addr)); }
DI bf16x8 ldsr128(unsigned addr) { return *(const LAS bf16x8*)(size_t)addr; }
DI void ldsw128(unsigned addr, u32x4 v) { *(LAS u32x4*)(size_t)addr = v; }
DI bf16x8 cat(s16x4 a, s16x4 b) { return (bf16x8){a[0], a[1], a[2], a[3], b[0], b[1], b[2], b[3]}; }
DI f32x16 zero16() { return (f32x16){0.f, 0.f, 0.f, 0.f, 0.f, 0.f, 0.f, 0.f, 0.f, 0.f, 0.f, 0.f, 0.f, 0.f, 0.f, 0.f}; }
DI u32x4 packacc(const f32x16& a, int s, float f) {
  return (u32x4){cvtpk(a[8 * s] * f, a[8 * s + 1] * f), cvtpk(a[8 * s + 2] * f, a[8 * s + 3] * f), cvtpk(a[8 * s + 4] * f, a[8 * s + 5] * f), cvtpk(a[8 * s + 6] * f, a[8 * s + 7] * f)};
}
DI u32x4 packacc1(const f32x16& a, int s) {
  return (u32x4){cvtpk(a[8 * s], a[8 * s + 1]), cvtpk(a[8 * s + 2], a[8 * s + 3]), cvtpk(a[8 * s + 4], a[8 * s + 5]), cvtpk(a[8 * s + 6], a[8 * s + 7])};
}
DI u32x4 swap32(u32x4 w) {
  auto s0 = __builtin_amdgcn_permlane32_swap(w[0], w[2], false, false);
  auto s1 = __builtin_amdgcn_permlane32_swap(w[1], w[3], false, false);
  return (u32x4){s0[0], s1[0], s0[1], s1[1]};
}
DI float max3f(float a, float b, float c) { return __builtin_fmaxf(__builtin_fmaxf(a, b), c); }
DI float red_max(float v) {
  auto q = __builtin_amdgcn_permlane32_swap(__float_as_uint(v), __float_as_uint(v), false, false);
  return __builtin_fmaxf(__uint_as_float(q[0]), __uint_as_float(q[1]));
}
DI float red_sum(float v) {
  auto q = __builtin_amdgcn_permlane32_swap(__float_as_uint(v), __float_as_uint(v), false, false);
  return __uint_as_float(q[0]) + __uint_as_float(q[1]);
}
template <int N> DI void waitbar() { asm volatile("s_waitcnt vmcnt(%0) lgkmcnt(0)\n\ts_barrier" ::"i"(N) : "memory"); SBAR(); }
DI void bar() { asm volatile("s_waitcnt lgkmcnt(0)\n\ts_barrier" ::: "memory"); SBAR(); }

struct Dma { rsrc_t srd; unsigned m0, so, sstep, v0; bool nt, k; };
template <int J> DI void dma_piece(const Dma& d) {
  const unsigned v = d.k ? (d.v0 ^ (64u * J)) : d.v0;
  const unsigned so = d.so + J * d.sstep;
  if (J == 0 || !M0ONCE) {
    if (d.nt) asm volatile("s_mov_b32 m0, %0\n\ts_nop 0\n\tbuffer_load_dwordx4 %1, %2, %3 offen offset:%4 nt lds" ::"s"(d.m0), "v"(v), "s"(d.srd), "s"(so), "i"(1024 * J) : "m0");
    else asm volatile("s_mov_b32 m0, %0\n\ts_nop 0\n\tbuffer_load_dwordx4 %1, %2, %3 offen offset:%4 lds" ::"s"(d.m0), "v"(v), "s"(d.srd), "s"(so), "i"(1024 * J) : "m0");
  } else {
    if (d.nt) asm volatile("buffer_load_dwordx4 %0, %1, %2 offen offset:%3 nt lds" ::"v"(v), "s"(d.srd), "s"(so), "i"(1024 * J));
    else asm volatile("buffer_load_dwordx4 %0, %1, %2 offen offset:%3 lds" ::"v"(v), "s"(d.srd), "s"(so), "i"(1024 * J));
  }
}
DI void dma_all(const Dma& d) { dma_piece<0>(d); dma_piece<1>(d); dma_piece<2>(d); dma_piece<3>(d); }
template <int K0, int KS> DI void dma_slot(const Dma& d, int k) {
  if (k == K0) dma_piece<0>(d);
  if (k == K0 + KS) dma_piece<1>(d);
  if (k == K0 + 2 * KS) dma_piece<2>(d);
  if (k == K0 + 3 * KS) dma_piece<3>(d);
}
template <int N, int PH> DI void dma_pos(const Dma& d, int k, bool early) {
  if (ORD == 0) { if (N >= 8) dma_slot<PH, 2>(d, k); else dma_slot<0, 1>(d, k); }
  else if (ORD == 1) { if (early) dma_slot<0, 1>(d, k); else dma_slot<N - 4, 1>(d, k); }
  else if (ORD == 2) { if (k == (early ? 0 : N - 1)) dma_all(d); }
  else if (ORD == 3) { const int k0 = early ? 0 : N - 2; if (k == k0) { dma_piece<0>(d); dma_piece<1>(d); } if (k == k0 + 1) { dma_piece<2>(d); dma_piece<3>(d); } }
  else if (ORD == 4) {
    if (PH == 1) { if (early) dma_slot<0, 1>(d, k); else dma_slot<N - 4, 1>(d, k); }
    else if (k == (early ? 0 : N - 1)) dma_all(d);
  }
}
DI void dma_n(const Dma& a, const Dma& b, int n) {
  if (n == 0) dma_piece<0>(a); if (n == 1) dma_piece<1>(a); if (n == 2) dma_piece<2>(a); if (n == 3) dma_piece<3>(a);
  if (n == 4) dma_piece<0>(b); if (n == 5) dma_piece<1>(b); if (n == 6) dma_piece<2>(b); if (n == 7) dma_piece<3>(b);
}

struct NoPre { DI void operator()() const {} };
template <int N, int D, int A, class RD, class MP, class MC, class FL, class PR = NoPre>
DI void pstep(RD rd, MP mprev, MC mcur, FL fill, PR pre = PR()) {
#pragma unroll
  for (int k = 0; k < N; ++k) {
    if (k < D) mprev(N - D + k); else mcur(k - D);
    SBAR();
    if (k == 0) {
      pre();
#pragma unroll
      for (int q = 0; q < A; ++q) rd(q);
    }
    if (k + A < N) rd(k + A);
    fill(k);
    SBAR();
  }
}

#define STAMP(i)

__global__ __launch_bounds__(512, 2) void mha_fused(const char* __restrict__ xA, const char* __restrict__ kA, const char* __restrict__ vA,
                                                    const char* __restrict__ wqA, const char* __restrict__ woA, const char* __restrict__ xB,
                                                    const char* __restrict__ kB, const char* __restrict__ vB, const char* __restrict__ wqB,
                                                    const char* __restrict__ woB, char* __restrict__ ws, char* __restrict__ out) {
  extern __shared__ __attribute__((aligned(16))) char lds[];
  const unsigned tid = threadIdx.x, lane = tid & 63, wid = rfl(tid >> 6), role = (ROLEMAP >> (4 * (wid >> 1))) & 3, qh = wid & 1;
  const unsigned xg = blockIdx.x & 7, p = xg >> 2, hg = xg & 3, b = blockIdx.x >> 3;
  const unsigned rot = b >> 3;
  const char* kc = (p ? kB : kA) + (size_t)(b * 32 + hg * 8) * 131072;
  const char* vc = (p ? vB : vA) + (size_t)(b * 32 + hg * 8) * 131072;
  const char* wq = p ? wqB : wqA;
  const char* wo = p ? woB : woA;
  const char* xp = (p ? xB : xA) + (size_t)b * 65536;
  const rsrc_t sK = __builtin_amdgcn_make_buffer_rsrc((void*)kc, (short)0, ABL_KV << 20, 0x00020000);
  const rsrc_t sV = __builtin_amdgcn_make_buffer_rsrc((void*)vc, (short)0, ABL_KV << 20, 0x00020000);
  const rsrc_t sQ = __builtin_amdgcn_make_buffer_rsrc((void*)wq, (short)0, ABL_W << 22, 0x00020000);
  const rsrc_t sO = __builtin_amdgcn_make_buffer_rsrc((void*)wo, (short)0, ABL_W << 22, 0x00020000);
  const rsrc_t sX = __builtin_amdgcn_make_buffer_rsrc((void*)xp, (short)0, 65536, 0x00020000);
  const unsigned ldsb = (unsigned)(size_t)lds;
  const unsigned g = lane >> 4, r16 = lane & 15, r32 = lane & 31, hh = lane >> 5;
  STAMP(0);
#pragma unroll
  for (int i = 0; i < 8; ++i) {
    const unsigned m0 = ldsb + role * REG + (1 + qh) * SLOT + i * 1024, so = (16 * role + 8 * qh + i) * 1024;
    const unsigned v = 16 * (lane ^ (8 * qh + i));
    if (NTM & 8) asm volatile("s_mov_b32 m0, %0\n\ts_nop 0\n\tbuffer_load_dwordx4 %1, %2, %3 offen nt lds" ::"s"(m0), "v"(v), "s"(sX), "s"(so) : "m0");
    else asm volatile("s_mov_b32 m0, %0\n\ts_nop 0\n\tbuffer_load_dwordx4 %1, %2, %3 offen lds" ::"s"(m0), "v"(v), "s"(sX), "s"(so) : "m0");
  }
  const unsigned swT = 16 * (r16 ^ (4 * g));
  const unsigned dq = g * 8192 + swT, dO = g * 1024 + swT, dV = g * 256 + swT, dK = g * 256 + 16 * (r16 ^ g);
  const unsigned mq = ldsb + qh * 4096;
  auto head = [&](unsigned u) -> unsigned { return (u + b) & 7u; };
  auto mkWq = [&](unsigned u, unsigned i, unsigned reg, unsigned slot, bool nt = false) -> Dma {
    return Dma{sQ, mq + reg * REG + slot * SLOT, (32 * ((i + 4 * rot) & 15) + 16 * qh) * 8192 + (8 * hg + head(u)) * 256, 4 * 8192 - 1024, dq, nt, false};
  };
  auto mkWo = [&](unsigned u, unsigned dblk, unsigned nq, unsigned reg, unsigned slot) -> Dma {
    return Dma{sO, mq + reg * REG + slot * SLOT, ((8 * hg + head(u)) * 128 + 32 * dblk + 16 * qh) * 1024 + ((nq + rot + hg) & 3) * 256, 4 * 1024 - 1024, dO, false, false};
  };
  auto stK = [&](int sg) -> Dma {
    const bool ok = (unsigned)sg < 128u;
    return Dma{sK, ok ? mq + REG + (sg & 3) * SLOT : NOLDS, ok ? head(sg >> 4) * 131072 + (sg & 15) * 8192 + qh * 4096 : OOR, 0, dK, true, true};
  };
  auto stV = [&](int sg) -> Dma {
    const int t = sg - 3; const bool ok = (unsigned)t < 128u;
    return Dma{sV, ok ? mq + 2 * REG + (sg & 3) * SLOT : NOLDS, ok ? head(t >> 4) * 131072 + (t & 15) * 8192 + qh * 4096 : OOR, 0, dV, true, false};
  };
  auto stQ = [&](int sg) -> Dma {
    const int t = sg + 17; const bool ok = t < 128;
    Dma d = mkWq(t >> 4, t & 15, 0, sg & 3);
    d.m0 = ok ? d.m0 : NOLDS; d.so = ok ? d.so : OOR;
    return d;
  };
  auto stO = [&](int sg) -> Dma {
    const int t = sg - 20;
    Dma d = mkWo(t >> 4, (t >> 2) & 3, t & 3, 3, sg & 3);
    d.m0 = t < 112 ? d.m0 : NOLDS; d.so = (unsigned)t < 112u ? d.so : OOR;
    return d;
  };
  auto genA = [&](int sg, bool odd) -> Dma { return odd ? stQ(sg) : stK(sg); };
  auto genB = [&](int sg, bool odd) -> Dma { return odd ? stO(sg) : stV(sg); };
  asm volatile("s_barrier" ::: "memory");
  dma_all(mkWq(0, role, role, 3, NTM & 1));
  asm volatile("s_barrier" ::: "memory");
  dma_all(mkWq(0, 4 + role, role, 0, NTM & 2));
  const unsigned tbl = ldsb + (4 * hh + (r16 >> 2)) * 256 + 64 * (r16 >> 2) + 32 * (g & 1) + 8 * (lane & 3);
  unsigned tbh = tbl + 2 * REG;
  PIN(tbh);
  bf16x8 fa[10];
  auto rdT = [&](int f, unsigned tb, unsigned off, int ct, int s) {
    const unsigned a = (tb ^ (64u * ct)) + (off + 4096 * s);
    fa[f] = cat(trd(a), trd(a + 2048));
  };
  auto nop = [&](int) {};
  unsigned xb = ldsb + XB + qh * 10240 + lane * 16;
  PIN(xb);
  auto prologue = [&](bf16x8* xf, const int R, auto extra) {
    waitbar<8>();
    {
      const unsigned row = 32 * qh + r32;
      const unsigned xa = ldsb + (row >> 4) * REG + (1 + ((row >> 3) & 1)) * SLOT + (row & 7) * 1024;
#pragma unroll
      for (int f = 0; f < 32; ++f) {
        const unsigned c = (2 * ((f + 8 * rot) & 31) + hh) ^ (row & 15);
        xf[f] = __builtin_bit_cast(bf16x8, swap32(__builtin_bit_cast(u32x4, ldsr128(xa + 16 * c))));
      }
    }
    waitbar<4>();
    if (R == 0) STAMP(1);
    f32x16 q0 = zero16();
#pragma unroll
    for (int j = 0; j < 4; ++j) {
      if (j == 0) { dma_all(mkWq(0, 8 + R, R, 1, NTM & 4)); dma_all(mkWq(0, 12 + R, R, 2)); }
      if (j >= 1) { const int sg = j - 2; dma_all(R == 0 ? stQ(sg) : R == 1 ? stK(sg) : R == 2 ? stV(sg) : stO(sg)); }
      pstep<8, 3, 2>([&](int m) { rdT(m, m < 4 ? tbl : tbh, ((m >> 1) & 1) * REG + ((j + 3) & 3) * SLOT, R, m & 1); },
                     [&](int m) { if (j > 0) q0 = mfma(fa[m], xf[8 * (j - 1) + m], q0); },
                     [&](int m) { q0 = mfma(fa[m], xf[8 * j + m], q0); }, nop);
      if (j == 0) extra();
      constexpr int X = 17 * WO_WARM;
      if (j == 0) { if (R == 2) waitbar<8 + X>(); else waitbar<8>(); }
      if (j == 1) { if (R == 2) waitbar<8 + X>(); else waitbar<8>(); }
      if (j == 2) { if (R == 2) waitbar<8 + X>(); else waitbar<8>(); }
      if (j == 3) { if (R == 2) bar(); else waitbar<8>(); }
    }
    q0 = mfma(fa[5], xf[29], q0); q0 = mfma(fa[6], xf[30], q0); q0 = mfma(fa[7], xf[31], q0);
    ldsw128(xb + 2048 * R + (R == 3 ? 2048 : 0), swap32(packacc(q0, 0, SC)));
    ldsw128(xb + 2048 * R + (R == 3 ? 2048 : 0) + 1024, swap32(packacc(q0, 1, SC)));
    SBAR();
  };
  auto noextra = [&]() {};
  const rsrc_t sP = __builtin_amdgcn_make_buffer_rsrc((void*)ws, (short)0, WS_FLAG, 0x00020000);

  if (role == 0) {
    bf16x8 xf[32];
    prologue(xf, 0, noextra);
    f32x16 qacc[4];
#pragma unroll
    for (int i = 0; i < 4; ++i) qacc[i] = zero16();
    auto mmQ = [&](int i, int m) { qacc[m >> 1] = mfma(fa[m], xf[2 * i + (m & 1)], (i == 0 && (m & 1) == 0) ? zero16() : qacc[m >> 1]); };
    auto putQ = [&](int k) {
      ldsw128(xb + 1024 * (k < 6 ? k : k + 2), swap32(packacc(qacc[k >> 1], k & 1, SC)));
    };
    for (int u = 1; u < 8; ++u) {
#pragma unroll
      for (int i = 0; i < 16; ++i) {
        const Dma da = stQ(16 * (u - 1) + i + 2);
        pstep<8, D1, A1>([&](int m) { rdT(m, tbl, ((i + 3) & 3) * SLOT, m >> 1, m & 1); },
                       [&](int m) { mmQ((i + 15) & 15, m); }, [&](int m) { mmQ(i, m); },
                       [&](int k) { dma_pos<8, 0>(da, k, (i & 1) == 0); if (i == 0 && u > 1) putQ(k); });
        waitbar<8>();
      }
    }
#pragma unroll
    for (int m = 8 - D1; m < 8; ++m) mmQ(15, m);
#pragma unroll
    for (int k = 0; k < 8; ++k) putQ(k);
    bar();
#pragma unroll
    for (int j = 0; j < 4; ++j) { dma_all(mkWo(7, 0, j + 1, 0, j)); bar(); bar(); bar(); bar(); }
    dma_all(mkWo(7, 1, 1, 1, 0)); dma_all(mkWo(7, 1, 2, 1, 1)); bar();
    dma_all(mkWo(7, 2, 1, 2, 0)); dma_all(mkWo(7, 3, 1, 3, 0)); dma_all(mkWo(7, 1, 3, 1, 2)); bar();
    dma_all(mkWo(7, 2, 2, 2, 1)); dma_all(mkWo(7, 3, 2, 3, 1)); dma_all(mkWo(7, 1, 0, 1, 3)); bar();
    dma_all(mkWo(7, 2, 3, 2, 2)); dma_all(mkWo(7, 3, 3, 3, 2)); dma_all(mkWo(7, 2, 0, 2, 3)); waitbar<28>();
    dma_all(mkWo(7, 3, 0, 3, 3)); waitbar<20>();
    waitbar<8>();
    waitbar<0>();
    bar();
  } else if (role == 1) {
    { bf16x8 xf[32]; prologue(xf, 1, noextra); }
    dma_all(stK(2)); waitbar<8>();
    const unsigned kb = ldsb + REG + r32 * 256 + 16 * (hh ^ (r32 & 15));
    unsigned pb = ldsb + PB + qh * 3072 + lane * 16;
    PIN(pb);
    f32x16 sb[4], negm = zero16();
    bf16x8 qf[8];
#pragma unroll
    for (int i = 0; i < 4; ++i) sb[i] = zero16();
#pragma unroll
    for (int i = 0; i < 8; ++i) { qf[i] = (bf16x8){0, 0, 0, 0, 0, 0, 0, 0}; fa[i] = qf[i]; }
    float l0 = 100.f, l1 = 100.f, al = 1.f, mx = 0.f, my = 0.f;
    auto mmK = [&](int i, int m) { sb[i & 3] = mfma(fa[m], qf[m], m == 0 ? (i < 2 ? zero16() : negm) : sb[i & 3]); };
    auto soft = [&](int i, int k, bool chk) {
      const int je = (i + 14) & 15, jm = (i + 15) & 15;
      f32x16& se = sb[je & 3];
      f32x16& sm = sb[jm & 3];
      auto g0 = [](int q) { return q < 4 ? 3 * q : 12 + 2 * (q - 4); };
      auto gn = [](int q) { return q < 4 ? 3 : 2; };
      if (k < 6) {
#pragma unroll
        for (int e = g0(k); e < g0(k) + gn(k); ++e) se[e] = __builtin_amdgcn_exp2f(se[e]);
      }
      if (k >= 1 && k < 7) {
#pragma unroll
        for (int e = g0(k - 1); e < g0(k - 1) + gn(k - 1); ++e) {
          if (e & 1) l1 = (e == 1 && je == 0) ? se[e] : l1 + se[e];
          else l0 = (e == 0 && je == 0) ? se[e] : l0 + se[e];
        }
        PIN(l0); PIN(l1);
      }
      if (k == 0) *(LAS float*)(size_t)(pb + (je & 1) * 6144 + 2048) = al;
      if (k == 3) ldsw128(pb + (je & 1) * 6144, packacc1(se, 0));
      if (k == 6) {
        ldsw128(pb + (je & 1) * 6144 + 1024, packacc1(se, 1));
        if (je == 15) *(LAS float*)(size_t)(pb + 6144 + 2052) = 1.0f / red_sum(l0 + l1);
      }
      if (chk) {
        if (k == 4) { mx = max3f(sm[0], sm[1], sm[2]); my = max3f(sm[3], sm[4], sm[5]); mx = max3f(mx, sm[6], sm[7]); }
        if (k == 5) { my = max3f(my, sm[8], sm[9]); mx = max3f(mx, sm[10], sm[11]); my = max3f(my, sm[12], sm[13]); }
        if (k == 6) { mx = max3f(mx, sm[14], sm[15]); mx = __builtin_fmaxf(mx, my); }
        if (k == 7) {
          al = 1.f;
          if (jm == 0) {
            const float d = red_max(mx);
#pragma unroll
            for (int e = 0; e < 16; ++e) { sm[e] -= d; sb[i & 3][e] -= d; negm[e] = -d; }
          } else if (__builtin_expect(__builtin_amdgcn_ballot_w64(mx > THR) != 0, 0)) {
            const float d = __builtin_fmaxf(red_max(mx), 0.f);
            al = __builtin_amdgcn_exp2f(-d);
            l0 *= al; l1 *= al;
#pragma unroll
            for (int e = 0; e < 16; ++e) { sm[e] -= d; if (jm != 15) { sb[i & 3][e] -= d; negm[e] -= d; } }
          }
        }
      }
    };
    if (PRIO & 2) __builtin_amdgcn_s_setprio(1);
    for (int u = 0; u < 8; ++u) {
      STAMP(2 + u);
#pragma unroll
      for (int i = 0; i < 16; ++i) {
        const Dma db = stK(16 * u + i + 3);
        pstep<8, D2, A2>([&](int m) { fa[m] = ldsr128((kb ^ (32u * m)) + (i & 3) * SLOT); },
                       [&](int m) { mmK((i + 15) & 15, m); }, [&](int m) { mmK(i, m); },
                       [&](int k) {
                         if (i == 0 && k == D2) {
#pragma unroll
                           for (int f = 8 - D2; f < 8; ++f) qf[f] = ldsr128(xb + 1024 * (f < 6 ? f : f + 2));
                         }
                         dma_pos<8, 1>(db, k, (i & 1) == 0);
                         soft(i, k, true);
                       },
                       [&]() {
                         if (i == 0) {
#pragma unroll
                           for (int f = 0; f < 8 - D2; ++f) qf[f] = ldsr128(xb + 1024 * f);
                         }
                       });
        waitbar<8>();
      }
    }
    STAMP(10);
#pragma unroll
    for (int m = 8 - D2; m < 8; ++m) mmK(15, m);
#pragma unroll
    for (int k = 0; k < 8; ++k) soft(0, k, true);
    bar();
#pragma unroll
    for (int k = 0; k < 8; ++k) soft(1, k, false);
    bar();
    bar(); bar(); bar(); bar(); bar();
    bar();
  } else {
    const unsigned grp = blockIdx.x & ~3u;
    auto stP = [&](unsigned ln, const f32x16& a, int gi, int ct, int q) {
      const unsigned so = ((grp + ((hg + gi) & 3)) * 3 + gi - 1) * 32768 + qh * 16384 + ct * 4096;
      __builtin_amdgcn_raw_buffer_store_b128(__builtin_bit_cast(u32x4, (f32x4){a[4 * q], a[4 * q + 1], a[4 * q + 2], a[4 * q + 3]}), sP, ln * 16 + q * 1024, so, 16);
    };
    auto putF = [&](unsigned ln, const f32x16& a, int ct) {
#pragma unroll
      for (int q = 0; q < 4; ++q)
        ldsw128(ldsb + ct * REG + qh * SLOT + q * 1024 + ln * 16, __builtin_bit_cast(u32x4, (f32x4){a[4 * q], a[4 * q + 1], a[4 * q + 2], a[4 * q + 3]}));
    };
    if (role == 2) {
#pragma unroll
      for (int i = 0; i < 10; ++i) ldsw128(xb + 1024 * i, (u32x4){0, 0, 0, 0});
      GAS unsigned long long* ep = (GAS unsigned long long*)(ws + WS_EPOCH + (blockIdx.x >> 2) * 128);
      unsigned long long tag;
      u32x4 wm[16];
      {
        bf16x8 xf[32];
        prologue(xf, 2, [&]() {
          if (WO_WARM) {
            asm volatile("global_load_dwordx2 %0, %1, off sc1" : "=&v"(tag) : "v"(ep) : "memory");
#pragma unroll
            for (int i = 0; i < 16; ++i) {
              const unsigned so = ((8 * hg + head(0)) * 128 + 64 * qh + 4 * i) * 1024 + ((rot + hg) & 3) * 256;
              asm volatile("buffer_load_dwordx4 %0, %1, %2, %3 offen" : "=&v"(wm[i]) : "v"(dO), "s"(sO), "s"(so) : "memory");
            }
          }
        });
      }
      if (!WO_WARM) {
        asm volatile("global_load_dwordx2 %0, %1, off sc1" : "=&v"(tag) : "v"(ep) : "memory");
#pragma unroll
        for (int i = 0; i < 16; ++i) wm[i] = (u32x4){0, 0, 0, 0};
      }
      bar();
      dma_all(stV(3)); bar();
      dma_all(stV(4)); bar();
      dma_all(stV(5));
      asm volatile("s_waitcnt vmcnt(8) lgkmcnt(0)\n\ts_barrier"
                   : "+v"(tag), "+v"(wm[0]), "+v"(wm[1]), "+v"(wm[2]), "+v"(wm[3]), "+v"(wm[4]), "+v"(wm[5]), "+v"(wm[6]), "+v"(wm[7]), "+v"(wm[8]), "+v"(wm[9]),
                     "+v"(wm[10]), "+v"(wm[11]), "+v"(wm[12]), "+v"(wm[13]), "+v"(wm[14]), "+v"(wm[15])::"memory");
      tag = ((unsigned long long)rfl((unsigned)(tag >> 32)) << 32) | rfl((unsigned)tag);
      tag = (tag ^ (tag >> 31)) * 0x9E3779B97F4A7C15ull + 0x632BE59BD9B4E019ull;
      unsigned pb = ldsb + PB + qh * 3072 + lane * 16;
      PIN(pb);
      f32x16 o[4], oa[4];
      bf16x8 pf[2][2], of[2][2];
#pragma unroll
      for (int i = 0; i < 4; ++i) { o[i] = zero16(); oa[i] = zero16(); }
#pragma unroll
      for (int i = 0; i < 10; ++i) fa[i] = (bf16x8){0, 0, 0, 0, 0, 0, 0, 0};
      pf[0][0] = pf[0][1] = pf[1][0] = pf[1][1] = of[0][0] = of[0][1] = of[1][0] = of[1][1] = fa[0];
      float linv = 0.f;
      auto mm3 = [&](int j, int m) {
        if (m < 8) o[m >> 1] = mfma(fa[m], pf[j & 1][m & 1], (j == 0 && (m & 1) == 0) ? zero16() : o[m >> 1]);
        else { const int iw = (j + 15) & 15; oa[iw & 3] = mfma(fa[m], of[(iw >> 2) & 1][m & 1], oa[iw & 3]); }
      };
      if (PRIO & 1) __builtin_amdgcn_s_setprio(1);
      for (int u = 0; u < 8; ++u) {
#pragma unroll
        for (int j = 0; j < 16; ++j) {
          const int iw = (j + 15) & 15;
          const Dma da = stV(16 * u + 6 + j);
          float alpha = 1.f;
          pstep<10, D3, A3>([&](int m) { if (m < 8) rdT(m, tbh, ((j + 3) & 3) * SLOT, m >> 1, m & 1); else rdT(m, tbh, REG + ((j + 3) & 3) * SLOT, 3, m & 1); },
                          [&](int m) { mm3((j + 15) & 15, m); }, [&](int m) { mm3(j, m); },
                          [&](int k) {
                            dma_pos<10, 1>(da, k, (j & 1) == 1);
                            if (j == 0 && k < 8) ldsw128(xb + 1024 * k, packacc(o[k >> 1], k & 1, linv));
                            if (j > 0 && k == D3 - 1) {
                              if (__builtin_expect(__builtin_amdgcn_ballot_w64(alpha != 1.f) != 0, 0)) {
#pragma unroll
                                for (int ct = 0; ct < 4; ++ct) o[ct] *= alpha;
                              }
                            }
                            if (j == 15 && k == 9) linv = *(const LAS float*)(size_t)(pb + 6144 + 2052);
                            if (k == 8 && (iw & 3) == 0) { of[(iw >> 2) & 1][0] = ldsr128(xb + 2048 * (iw >> 2)); of[(iw >> 2) & 1][1] = ldsr128(xb + 2048 * (iw >> 2) + 1024); }
                          },
                          [&]() {
                            if (j > 0) alpha = *(const LAS float*)(size_t)(pb + (j & 1) * 6144 + 2048);
                            pf[j & 1][0] = ldsr128(pb + (j & 1) * 6144); pf[j & 1][1] = ldsr128(pb + (j & 1) * 6144 + 1024);
                          });
          waitbar<8>();
        }
      }
#pragma unroll
      for (int m = 10 - D3; m < 10; ++m) mm3(15, m);
      rdT(8, tbh, REG + 3 * SLOT, 3, 0); rdT(9, tbh, REG + 3 * SLOT, 3, 1);
      bf16x8 og[8];
#pragma unroll
      for (int k = 0; k < 8; ++k) { const u32x4 w = packacc(o[k >> 1], k & 1, linv); ldsw128(xb + 1024 * k, w); og[k] = __builtin_bit_cast(bf16x8, w); }
      mm3(0, 8); mm3(0, 9);
      bar();
      unsigned ln;
      asm volatile("v_mbcnt_lo_u32_b32 %0, -1, 0\n\tv_mbcnt_hi_u32_b32 %0, -1, %0" : "=v"(ln));
      const unsigned tl2 = tbh - 2 * REG;
#pragma unroll
      for (int j = 0; j < 4; ++j) {
        const int gi = (j + 1) & 3;
#pragma unroll
        for (int m = 0; m < 8; ++m) rdT(m, m < 4 ? tl2 : tbh, ((m >> 1) & 1) * REG + j * SLOT, 3, m & 1);
#pragma unroll
        for (int m = 0; m < 8; ++m) oa[gi] = mfma(fa[m], og[m], oa[gi]);
        if (j < 3) {
#pragma unroll
          for (int q = 0; q < 4; ++q) stP(ln, oa[gi], gi, 3, q);
          bar();
        }
      }
      putF(ln, oa[0], 3);
      waitbar<0>();
      if (qh == 0) {
        if (ln < 3)
          __hip_atomic_store((GAS unsigned long long*)(ws + WS_FLAG + ((grp + ((hg + ln + 1) & 3)) * 4 + ln) * 128), tag, __ATOMIC_RELAXED, __HIP_MEMORY_SCOPE_AGENT);
        GAS unsigned long long* fl = (GAS unsigned long long*)(ws + WS_FLAG + (blockIdx.x * 4 + (ln < 3 ? ln : 0)) * 128);
        for (unsigned spins = 0; spins < (1u << 22); ++spins) {
          const unsigned long long v = __hip_atomic_load(fl, __ATOMIC_RELAXED, __HIP_MEMORY_SCOPE_AGENT);
          if (__all(v == tag)) break;
          __builtin_amdgcn_s_sleep(1);
        }
        if (hg == 0 && ln == 0) __hip_atomic_store(ep, tag, __ATOMIC_RELAXED, __HIP_MEMORY_SCOPE_AGENT);
      }
    } else {
      { bf16x8 xf[32]; prologue(xf, 3, noextra); }
      dma_all(stO(2)); waitbar<8>();
      for (int s = 0; s < 17; ++s) waitbar<0>();
      dma_all(stO(20)); bar(); dma_all(stO(21)); bar(); dma_all(stO(22)); waitbar<8>();
      f32x16 oa[4][3];
      bf16x8 of[2][2];
#pragma unroll
      for (int i = 0; i < 12; ++i) oa[i / 3][i % 3] = zero16();
#pragma unroll
      for (int i = 0; i < 6; ++i) fa[i] = (bf16x8){0, 0, 0, 0, 0, 0, 0, 0};
      of[0][0] = of[0][1] = of[1][0] = of[1][1] = fa[0];
      auto mm4 = [&](int nq, int par, int m) { oa[nq][m >> 1] = mfma(fa[m], of[par][m & 1], oa[nq][m >> 1]); };
      for (int u = 0; u < 7; ++u) {
#pragma unroll
        for (int i = 0; i < 16; ++i) {
          const int ip = (i + 15) & 15, sg = 16 * u + 23 + i;
          const Dma db = stO(sg);
          pstep<6, D4, A4>([&](int m) { rdT(m, tbh, REG + (i & 3) * SLOT, m >> 1, m & 1); },
                         [&](int m) { mm4(ip & 3, (ip >> 2) & 1, m); }, [&](int m) { mm4(i & 3, (i >> 2) & 1, m); },
                         [&](int k) { dma_pos<6, 0>(db, k, (i & 1) == 1); },
                         [&]() { if ((i & 3) == 0) { of[(i >> 2) & 1][0] = ldsr128(xb + 2048 * (i >> 2)); of[(i >> 2) & 1][1] = ldsr128(xb + 2048 * (i >> 2) + 1024); } });
          waitbar<8>();
        }
      }
      STAMP(11);
      unsigned ln;
      asm volatile("v_mbcnt_lo_u32_b32 %0, -1, 0\n\tv_mbcnt_hi_u32_b32 %0, -1, %0" : "=v"(ln));
      const unsigned tl2 = tbh - 2 * REG;
#pragma unroll
      for (int j = 0; j < 4; ++j) {
        const int gi = (j + 1) & 3;
#pragma unroll
        for (int q = 0; q < 4; ++q) {
          of[q & 1][0] = ldsr128(xb + 2048 * q); of[q & 1][1] = ldsr128(xb + 2048 * q + 1024);
          pstep<6, D4, A4>([&](int m) { rdT(m, q < 2 ? tl2 : tbh, (q & 1) * REG + j * SLOT, m >> 1, m & 1); },
                         [&](int m) { if (q > 0) mm4(gi, (q - 1) & 1, m); else mm4(j == 0 ? 3 : j, 1, m); },
                         [&](int m) { mm4(gi, q & 1, m); },
                         [&](int k) {
                           if (j > 0 && q < 2) { const int n = 6 * q + k; stP(ln, oa[j][n >> 2], j, n >> 2, n & 3); }
                         });
          if (j == 3 && q == 1) waitbar<0>();
        }
        if (j < 3) bar();
      }
#pragma unroll
      for (int m = 6 - D4; m < 6; ++m) mm4(0, 1, m);
#pragma unroll
      for (int ct = 0; ct < 3; ++ct) putF(ln, oa[0][ct], ct);
      STAMP(12);
    }
  }
  waitbar<0>();
  {
    unsigned ln;
    asm volatile("v_mbcnt_lo_u32_b32 %0, -1, 0\n\tv_mbcnt_hi_u32_b32 %0, -1, %0" : "=v"(ln));
    u32x4 in[12];
#pragma unroll
    for (int j = 0; j < 3; ++j)
#pragma unroll
      for (int q = 0; q < 4; ++q)
        in[4 * j + q] = __builtin_amdgcn_raw_buffer_load_b128(sP, ln * 16 + q * 1024, (blockIdx.x * 3 + j) * 32768 + qh * 16384 + role * 4096, 16);
    f32x16 a;
#pragma unroll
    for (int q = 0; q < 4; ++q) {
      const f32x4 own = __builtin_bit_cast(f32x4, ldsr128(ldsb + role * REG + qh * SLOT + q * 1024 + ln * 16));
#pragma unroll
      for (int e = 0; e < 4; ++e) a[4 * q + e] = ((own[e] + __uint_as_float(in[q][e])) + __uint_as_float(in[4 + q][e])) + __uint_as_float(in[8 + q][e]);
    }
    char* orow = out + ((size_t)b * 64 + 32 * qh + (ln & 31)) * 2048 + p * 1024 + ((rot + hg) & 3) * 256 + role * 64 + (ln >> 5) * 16;
    *(u32x4*)(orow) = swap32(packacc1(a, 0));
    *(u32x4*)(orow + 32) = swap32(packacc1(a, 1));
  }
}

extern "C" void kernel_launch(void* const* d_in, const int* in_sizes, int n_in, void* d_out, int out_size, void* d_ws, size_t ws_size,
                              hipStream_t stream) {
  (void)in_sizes; (void)n_in; (void)out_size; (void)ws_size;
  (void)hipFuncSetAttribute((const void*)mha_fused, hipFuncAttributeMaxDynamicSharedMemorySize, LDS_BYTES);
  mha_fused<<<256, 512, LDS_BYTES, stream>>>((const char*)d_in[0], (const char*)d_in[1], (const char*)d_in[2], (const char*)d_in[3],
                                             (const char*)d_in[4], (const char*)d_in[5], (const char*)d_in[6], (const char*)d_in[7],
                                             (const char*)d_in[8], (const char*)d_in[9], (char*)d_ws, (char*)d_out);
}
```

```cpp
#include <hip/hip_runtime.h>
#include <hip/hip_bf16.h>

typedef __amdgpu_buffer_rsrc_t rsrc_t;
using bf16x8 = __attribute__((ext_vector_type(8))) short;
using s16x4 = __attribute__((ext_vector_type(4))) short;
using f32x4 = __attribute__((ext_vector_type(4))) float;
using f32x2 = __attribute__((ext_vector_type(2))) float;
using u32x4 = __attribute__((ext_vector_type(4))) unsigned;
using bf16x2_t = __attribute__((ext_vector_type(2))) __bf16;
typedef short v4i16_t __attribute__((ext_vector_type(4)));
#define LAS __attribute__((address_space(3)))
#define GAS __attribute__((address_space(1)))
#define DI __device__ __forceinline__
#define SBAR() __builtin_amdgcn_sched_barrier(0)
#define PIN(x) asm volatile("" : "+v"(x))

constexpr int SLOT = 8192, REG = 32768;
constexpr int XB = 4 * REG;
constexpr int PB = XB + 2 * 10240;
constexpr int LDS_BYTES = PB + 4 * 3072;
constexpr unsigned WS_FLAG = 32u << 20, WS_EPOCH = 33u << 20;
constexpr float SC = 0.08838834764831845f * 1.4426950408889634f;
constexpr unsigned OOR = 0x80000000u;
constexpr unsigned NOLDS = 0x30000u;
#define THR 24.0f
#define ABL_KV 1
#define ABL_W 1
#define KVNT true
#define PBF16 1
#define DRQ 1
#define WARM 1
#define WS0 2
#define ORD 0
#define ROLEMAP 0x3210
#define D1 3
#define D3 3
#define D4 2
#define AH 2

DI unsigned rfl(unsigned x) { return __builtin_amdgcn_readfirstlane(x); }
DI unsigned cvtpk(float lo, float hi) { f32x2 v = {lo, hi}; return __builtin_bit_cast(unsigned, __builtin_convertvector(v, bf16x2_t)); }
DI f32x4 mfma(bf16x8 a, bf16x8 b, f32x4 c) { return __builtin_amdgcn_mfma_f32_16x16x32_bf16(a, b, c, 0, 0, 0); }
DI s16x4 trd(unsigned addr) { return __builtin_bit_cast(s16x4, __builtin_amdgcn_ds_read_tr16_b64_v4i16((LAS v4i16_t*)(size_t)addr)); }
DI bf16x8 ldsr128(unsigned addr) { return *(const LAS bf16x8*)(size_t)addr; }
DI void ldsw128(unsigned addr, u32x4 v) { *(LAS u32x4*)(size_t)addr = v; }
DI bf16x8 cat(s16x4 a, s16x4 b) { return (bf16x8){a[0], a[1], a[2], a[3], b[0], b[1], b[2], b[3]}; }
DI f32x4 zero4() { return (f32x4){0.f, 0.f, 0.f, 0.f}; }
DI bf16x8 zero8() { return (bf16x8){0, 0, 0, 0, 0, 0, 0, 0}; }
DI u32x4 pack8(f32x4 a, f32x4 b) { return (u32x4){cvtpk(a[0], a[1]), cvtpk(a[2], a[3]), cvtpk(b[0], b[1]), cvtpk(b[2], b[3])}; }
DI u32x4 swap16(u32x4 w) {
  auto s0 = __builtin_amdgcn_permlane16_swap(w[0], w[2], false, false);
  auto s1 = __builtin_amdgcn_permlane16_swap(w[1], w[3], false, false);
  return (u32x4){s0[0], s1[0], s0[1], s1[1]};
}
DI float max3f(float a, float b, float c) { return __builtin_fmaxf(__builtin_fmaxf(a, b), c); }
DI float red_max(float v) {
  auto r = __builtin_amdgcn_permlane16_swap(__float_as_uint(v), __float_as_uint(v), false, false);
  v = __builtin_fmaxf(__uint_as_float(r[0]), __uint_as_float(r[1]));
  auto q = __builtin_amdgcn_permlane32_swap(__float_as_uint(v), __float_as_uint(v), false, false);
  return __builtin_fmaxf(__uint_as_float(q[0]), __uint_as_float(q[1]));
}
DI float red_sum(float v) {
  auto r = __builtin_amdgcn_permlane16_swap(__float_as_uint(v), __float_as_uint(v), false, false);
  v = __uint_as_float(r[0]) + __uint_as_float(r[1]);
  auto q = __builtin_amdgcn_permlane32_swap(__float_as_uint(v), __float_as_uint(v), false, false);
  return __uint_as_float(q[0]) + __uint_as_float(q[1]);
}
template <int N> DI void waitbar() { asm volatile("s_waitcnt vmcnt(%0) lgkmcnt(0)\n\ts_barrier" ::"i"(N) : "memory"); SBAR(); }
DI void bar() { asm volatile("s_waitcnt lgkmcnt(0)\n\ts_barrier" ::: "memory"); SBAR(); }

struct Dma { rsrc_t srd; unsigned m0, so, sstep, v0; bool nt; int kind; };
template <int J> DI void dma_piece(const Dma& d) {
  const unsigned v = d.v0 ^ (d.kind == 1 ? 64u * J : d.kind == 0 ? 128u * (J & 1) : 128u * (J >> 1));
  const unsigned so = d.so + J * d.sstep;
  if (J == 0) {
    if (d.nt) asm volatile("s_mov_b32 m0, %0\n\ts_nop 0\n\tbuffer_load_dwordx4 %1, %2, %3 offen offset:%4 nt lds" ::"s"(d.m0), "v"(v), "s"(d.srd), "s"(so), "i"(1024 * J) : "m0");
    else asm volatile("s_mov_b32 m0, %0\n\ts_nop 0\n\tbuffer_load_dwordx4 %1, %2, %3 offen offset:%4 lds" ::"s"(d.m0), "v"(v), "s"(d.srd), "s"(so), "i"(1024 * J) : "m0");
  } else {
    if (d.nt) asm volatile("buffer_load_dwordx4 %0, %1, %2 offen offset:%3 nt lds" ::"v"(v), "s"(d.srd), "s"(so), "i"(1024 * J));
    else asm volatile("buffer_load_dwordx4 %0, %1, %2 offen offset:%3 lds" ::"v"(v), "s"(d.srd), "s"(so), "i"(1024 * J));
  }
}
DI void dma_all(const Dma& d) { dma_piece<0>(d); dma_piece<1>(d); dma_piece<2>(d); dma_piece<3>(d); }
template <int K0, int KS> DI void dma_slot(const Dma& d, int k) {
  if (k == K0) dma_piece<0>(d);
  if (k == K0 + KS) dma_piece<1>(d);
  if (k == K0 + 2 * KS) dma_piece<2>(d);
  if (k == K0 + 3 * KS) dma_piece<3>(d);
}
template <int N, int PH> DI void dma_pos(const Dma& d, int k, bool early) {
  if (ORD == 0) { if (N >= 8) dma_slot<PH, 2>(d, k); else dma_slot<0, 1>(d, k); }
  else { if (early) dma_slot<0, 1>(d, k); else dma_slot<N - 4, 1>(d, k); }
}

struct NoPre { DI void operator()() const {} };
template <int N, int D, int A, class RD, class MP, class MC, class FL, class PR = NoPre>
DI void pstep(RD rd, MP mprev, MC mcur, FL fill, PR pre = PR()) {
#pragma unroll
  for (int k = 0; k < N; ++k) {
#pragma unroll
    for (int h = 0; h < 2; ++h) {
      if (k < D) mprev(N - D + k, h); else mcur(k - D, h);
      SBAR();
      if (k == 0) {
        if (h == 0) pre();
#pragma unroll
        for (int q = h; q < A; q += 2) { rd(q, 0); rd(q, 1); }
      }
      if (k + A < N) rd(k + A, h);
      fill(k, h);
      SBAR();
    }
  }
}

#define STAMP(i)

__global__ __launch_bounds__(512, 2) void mha_fused(const char* __restrict__ xA, const char* __restrict__ kA, const char* __restrict__ vA,
                                                    const char* __restrict__ wqA, const char* __restrict__ woA, const char* __restrict__ xB,
                                                    const char* __restrict__ kB, const char* __restrict__ vB, const char* __restrict__ wqB,
                                                    const char* __restrict__ woB, char* __restrict__ ws, char* __restrict__ out) {
  extern __shared__ __attribute__((aligned(16))) char lds[];
  unsigned lane = threadIdx.x & 63;
  const unsigned tid = threadIdx.x, wid = rfl(tid >> 6), role = (ROLEMAP >> (4 * (wid >> 1))) & 3, qh = wid & 1;
  const unsigned xg = blockIdx.x & 7, p = xg >> 2, hg = xg & 3, b = blockIdx.x >> 3;
  const unsigned rot = b >> 3;
  const char* kc = (p ? kB : kA) + (size_t)(b * 32 + hg * 8) * 131072;
  const char* vc = (p ? vB : vA) + (size_t)(b * 32 + hg * 8) * 131072;
  const char* wq = p ? wqB : wqA;
  const char* wo = p ? woB : woA;
  const char* xp = (p ? xB : xA) + (size_t)b * 65536;
  const rsrc_t sK = __builtin_amdgcn_make_buffer_rsrc((void*)kc, (short)0, ABL_KV << 20, 0x00020000);
  const rsrc_t sV = __builtin_amdgcn_make_buffer_rsrc((void*)vc, (short)0, ABL_KV << 20, 0x00020000);
  const rsrc_t sQ = __builtin_amdgcn_make_buffer_rsrc((void*)wq, (short)0, ABL_W << 22, 0x00020000);
  const rsrc_t sO = __builtin_amdgcn_make_buffer_rsrc((void*)wo, (short)0, ABL_W << 22, 0x00020000);
  const rsrc_t sX = __builtin_amdgcn_make_buffer_rsrc((void*)xp, (short)0, 65536, 0x00020000);
  const unsigned ldsb = (unsigned)(size_t)lds;
  unsigned g, r, dq, dO, dV, dK, tq, tbh, xb;
  auto setup = [&](bool opaque) {
    if (opaque) asm volatile("v_mbcnt_lo_u32_b32 %0, -1, 0\n\tv_mbcnt_hi_u32_b32 %0, -1, %0" : "=v"(lane));
    g = lane >> 4; r = lane & 15;
    const unsigned swT = 16 * (r ^ (2 * g));
    dq = g * 8192 + swT; dO = g * 1024 + swT; dV = g * 256 + swT; dK = g * 256 + 16 * (r ^ g);
    const unsigned q4 = r >> 2;
    tq = ldsb + (8 * g + q4) * 256 + 32 * (q4 | ((g & 1) << 2)) + 8 * (lane & 3);
    tbh = ldsb + 2 * REG + (lane >> 2) * 256 + 32 * ((lane >> 2) & 7) + 8 * (lane & 3);
    xb = ldsb + XB + qh * 10240 + lane * 16;
    if (opaque) { PIN(tbh); PIN(xb); }
  };
  setup(false);
  STAMP(0);
#pragma unroll
  for (int i = 0; i < 8; ++i) {
    const unsigned m0 = ldsb + role * REG + (1 + qh) * SLOT + i * 1024, so = (16 * role + 8 * qh + i) * 1024;
    const unsigned v = 16 * (((lane ^ (8 * qh + i)) + 16 * rot) & 63);
    asm volatile("s_mov_b32 m0, %0\n\ts_nop 0\n\tbuffer_load_dwordx4 %1, %2, %3 offen lds" ::"s"(m0), "v"(v), "s"(sX), "s"(so) : "m0");
  }
  const unsigned mq = ldsb + qh * 4096;
  auto head = [&](unsigned u) -> unsigned { return (u + b) & 7u; };
  auto mkWq = [&](unsigned u, unsigned i, unsigned reg, unsigned slot, bool nt = false) -> Dma {
    return Dma{sQ, mq + reg * REG + slot * SLOT, (32 * ((i + 4 * rot) & 15) + 16 * qh) * 8192 + (8 * hg + head(u)) * 256, 4 * 8192 - 1024, dq, nt, 2};
  };
  auto mkWo = [&](unsigned u, unsigned dblk, unsigned nq, unsigned reg, unsigned slot) -> Dma {
    return Dma{sO, mq + reg * REG + slot * SLOT, ((8 * hg + head(u)) * 128 + 32 * dblk + 16 * qh) * 1024 + ((nq + rot + hg) & 3) * 256, 4 * 1024 - 1024, dO, false, 0};
  };
  auto stK = [&](int sg) -> Dma {
    const bool ok = (unsigned)sg < 128u;
    return Dma{sK, ok ? mq + REG + (sg & 3) * SLOT : NOLDS, ok ? head(sg >> 4) * 131072 + (sg & 15) * 8192 + qh * 4096 : OOR, 0, dK, KVNT, 1};
  };
  auto stV = [&](int sg) -> Dma {
    const int t = sg - 3; const bool ok = (unsigned)t < 128u;
    return Dma{sV, ok ? mq + 2 * REG + (sg & 3) * SLOT : NOLDS, ok ? head(t >> 4) * 131072 + (t & 15) * 8192 + qh * 4096 : OOR, 0, dV, KVNT, 0};
  };
  auto stQ = [&](int sg) -> Dma {
    const int t = sg + 17; const bool ok = t < 128;
    Dma d = mkWq(t >> 4, t & 15, 0, sg & 3);
    d.m0 = ok ? d.m0 : NOLDS; d.so = ok ? d.so : OOR;
    return d;
  };
  auto stO = [&](int sg) -> Dma {
    const int t = sg - 20;
    Dma d = mkWo(t >> 4, (t >> 2) & 3, t & 3, 3, sg & 3);
    d.m0 = t < 112 ? d.m0 : NOLDS; d.so = (unsigned)t < 112u ? d.so : OOR;
    return d;
  };
  { Dma w = mkWo(0, role, 0, role, 0); if (WARM < 4) { w.so = OOR; w.m0 = NOLDS; } w.nt = WARM == 5; dma_all(w); }
  asm volatile("s_barrier" ::: "memory");
  dma_all(mkWq(0, role, role, 3, true));
  s16x4 fh[10][2];
  auto FA = [&](int f) { return cat(fh[f][0], fh[f][1]); };
  auto rdT = [&](int f, int h, unsigned tb, unsigned off, int ct) { fh[f][h] = trd((tb ^ (32u * ct)) + (off + 4096 * h)); };
  auto rdQ = [&](int f, int h, unsigned tb, unsigned off, int ct) { fh[f][h] = trd((tb ^ (32u * ct)) + (off + 1024 * h)); };
  auto nop = [&](int, int) {};
  auto zfa = [&]() {
#pragma unroll
    for (int i = 0; i < 10; ++i) fh[i][0] = fh[i][1] = (s16x4){0, 0, 0, 0};
  };
  auto prologue = [&](bf16x8 (*xf)[2], const int R, auto extra) {
    waitbar<8>();
    {
      const Dma f4 = mkWq(0, 4 + R, R, 0);
      const unsigned xa = ldsb + 2 * qh * REG + (1 + (r >> 3)) * SLOT + (r & 7) * 1024 + 16 * (g ^ r);
#pragma unroll
      for (int i = 0; i < 16; ++i) {
        xf[i][0] = ldsr128((xa ^ (64u * (i & 3))) + 256 * (i >> 2));
        xf[i][1] = ldsr128((xa ^ (64u * (i & 3))) + 256 * (i >> 2) + REG);
        dma_slot<3, 4>(f4, i);
      }
    }
    waitbar<4>();
    if (R == 0) STAMP(1);
    f32x4 q0[2][2] = {{zero4(), zero4()}, {zero4(), zero4()}};
    unsigned tqh = tq + 2 * REG;
    PIN(tqh);
    auto mm0 = [&](int j, int m, int t) { q0[m & 1][t] = mfma(FA(m), xf[4 * j + (m >> 1)][t], q0[m & 1][t]); };
#pragma unroll
    for (int j = 0; j < 4; ++j) {
      if (j == 0) { dma_all(mkWq(0, 8 + R, R, 1)); dma_all(mkWq(0, 12 + R, R, 2)); }
      if (j >= 1) { const int sg = j - 2; dma_all(R == 0 ? stQ(sg) : R == 1 ? stK(sg) : R == 2 ? stV(sg) : stO(sg)); }
      pstep<8, 3, 2>([&](int m, int h) { rdQ(m, h, m < 4 ? tq : tqh, ((m >> 1) & 1) * REG + ((j + 3) & 3) * SLOT, 2 * R + (m & 1)); },
                     [&](int m, int t) { if (j > 0) mm0(j - 1, m, t); }, [&](int m, int t) { mm0(j, m, t); }, nop);
      if (j == 0) extra();
      if (R == 2) { if (j < 3) waitbar<25>(); else bar(); } else waitbar<8>();
    }
#pragma unroll
    for (int m = 10; m < 16; ++m) mm0(3, m >> 1, m & 1);
    ldsw128(xb + 2048 * R + (R == 3 ? 2048 : 0), swap16(pack8(q0[0][0] * SC, q0[1][0] * SC)));
    ldsw128(xb + 2048 * R + (R == 3 ? 2048 : 0) + 1024, swap16(pack8(q0[0][1] * SC, q0[1][1] * SC)));
    SBAR();
  };
  auto noextra = [&]() {};
  const rsrc_t sP = __builtin_amdgcn_make_buffer_rsrc((void*)ws, (short)0, WS_FLAG, 0x00020000);

  if (role == 0) {
    setup(true);
    bf16x8 xf[16][2];
    prologue(xf, 0, noextra);
    f32x4 qacc[8][2];
#pragma unroll
    for (int i = 0; i < 8; ++i) qacc[i][0] = qacc[i][1] = zero4();
    auto mmQ = [&](int i, int m, int t) { qacc[m][t] = mfma(FA(m), xf[i][t], i == 0 ? zero4() : qacc[m][t]); };
    auto putQ = [&](int k) {
      ldsw128(xb + 1024 * (k < 6 ? k : k + 2), swap16(pack8(qacc[k & 6][k & 1] * SC, qacc[(k & 6) + 1][k & 1] * SC)));
    };
    for (int u = 1; u < 8; ++u) {
#pragma unroll
      for (int i = 0; i < 16; ++i) {
        const Dma da = stQ(16 * (u - 1) + i + 2);
        pstep<8, D1, AH>([&](int m, int h) { rdQ(m, h, tq, ((i + 3) & 3) * SLOT, m); },
                         [&](int m, int t) { mmQ((i + 15) & 15, m, t); }, [&](int m, int t) { mmQ(i, m, t); },
                         [&](int k, int h) { if (h == 0) dma_pos<8, 0>(da, k, (i & 1) == 0); if (h == 1 && i == 0 && u > 1) putQ(k); });
        waitbar<8>();
      }
    }
#pragma unroll
    for (int m = 8 - D1; m < 8; ++m) { mmQ(15, m, 0); mmQ(15, m, 1); }
#pragma unroll
    for (int k = 0; k < 8; ++k) putQ(k);
    bar();
#pragma unroll
    for (int j = 0; j < 4; ++j) { dma_all(mkWo(7, 0, j + 1, 0, j)); bar(); bar(); bar(); bar(); }
    dma_all(mkWo(7, 1, 1, 1, 0)); dma_all(mkWo(7, 1, 2, 1, 1)); bar();
    dma_all(mkWo(7, 2, 1, 2, 0)); dma_all(mkWo(7, 3, 1, 3, 0)); dma_all(mkWo(7, 1, 3, 1, 2)); bar();
    dma_all(mkWo(7, 2, 2, 2, 1)); dma_all(mkWo(7, 3, 2, 3, 1)); dma_all(mkWo(7, 1, 0, 1, 3)); bar();
    dma_all(mkWo(7, 2, 3, 2, 2)); dma_all(mkWo(7, 3, 3, 3, 2)); dma_all(mkWo(7, 2, 0, 2, 3)); waitbar<28>();
    dma_all(mkWo(7, 3, 0, 3, 3)); waitbar<20>();
    waitbar<8>();
    waitbar<0>();
    bar();
  } else if (role == 1) {
    setup(true);
    { bf16x8 xf[16][2]; prologue(xf, 1, noextra); }
    dma_all(stK(2)); waitbar<8>();
    const unsigned sig = ((g & 1) << 1) | (g >> 1);
    const unsigned kb = ldsb + REG + r * 256 + 16 * (sig ^ r);
    unsigned pb = ldsb + PB + qh * 3072 + lane * 16;
    PIN(pb);
    f32x4 sb[4][2][2], negm[2] = {zero4(), zero4()};
    bf16x8 qf[4][2], fa[8];
#pragma unroll
    for (int i = 0; i < 16; ++i) sb[i >> 2][(i >> 1) & 1][i & 1] = zero4();
#pragma unroll
    for (int i = 0; i < 8; ++i) { qf[i >> 1][i & 1] = zero8(); fa[i] = zero8(); }
    float l[2] = {0.f, 0.f}, mx[2] = {0.f, 0.f};
    f32x2 al = {1.f, 1.f};
    auto mmK = [&](int i, int m, int t) {
      sb[i & 3][m >> 2][t] = mfma(fa[m], qf[m & 3][t], (m & 3) == 0 ? (i < 2 ? zero4() : negm[t]) : sb[i & 3][m >> 2][t]);
    };
    auto soft = [&](int i, int k, int h, bool chk) {
      const int je = (i + 14) & 15, jm = (i + 15) & 15;
      auto& se = sb[je & 3];
      auto& sm = sb[jm & 3];
      auto g0 = [](int q) { return q < 4 ? 3 * q : 12 + 2 * (q - 4); };
      auto gn = [](int q) { return q < 4 ? 3 : 2; };
      if (h == 0 && k < 6) {
#pragma unroll
        for (int e = g0(k); e < g0(k) + gn(k); ++e) se[(e >> 2) & 1][e >> 3][e & 3] = __builtin_amdgcn_exp2f(se[(e >> 2) & 1][e >> 3][e & 3]);
      }
      if (h == 0) return;
      if (k >= 1 && k < 7) {
#pragma unroll
        for (int e = g0(k - 1); e < g0(k - 1) + gn(k - 1); ++e) {
          const float v = se[(e >> 2) & 1][e >> 3][e & 3];
          l[e >> 3] = ((e & 7) == 0 && je == 0) ? v : l[e >> 3] + v;
        }
        PIN(l[0]); PIN(l[1]);
      }
      if (k == 0) *(LAS f32x2*)(size_t)(pb + (je & 1) * 6144 + 2048) = al;
      if (k == 3) ldsw128(pb + (je & 1) * 6144, pack8(se[0][0], se[1][0]));
      if (k == 6) {
        ldsw128(pb + (je & 1) * 6144 + 1024, pack8(se[0][1], se[1][1]));
        if (je == 15) *(LAS f32x2*)(size_t)(pb + 6144 + 2056) = (f32x2){1.0f / red_sum(l[0]), 1.0f / red_sum(l[1])};
      }
      if (chk) {
        if (k == 3) { mx[0] = max3f(sm[0][0][0], sm[0][0][1], sm[0][0][2]); mx[1] = max3f(sm[0][1][0], sm[0][1][1], sm[0][1][2]); }
        if (k == 4) { mx[0] = max3f(mx[0], sm[0][0][3], sm[1][0][0]); mx[1] = max3f(mx[1], sm[0][1][3], sm[1][1][0]); }
        if (k == 5) { mx[0] = max3f(mx[0], sm[1][0][1], sm[1][0][2]); mx[1] = max3f(mx[1], sm[1][1][1], sm[1][1][2]); }
        if (k == 6) { mx[0] = __builtin_fmaxf(mx[0], sm[1][0][3]); mx[1] = __builtin_fmaxf(mx[1], sm[1][1][3]); }
        if (k == 7) {
          al = (f32x2){1.f, 1.f};
          if (jm == 0) {
#pragma unroll
            for (int t = 0; t < 2; ++t) {
              const float d = red_max(mx[t]);
              sm[0][t] -= d; sm[1][t] -= d; sb[i & 3][0][t] -= d; sb[i & 3][1][t] -= d;
              negm[t] = (f32x4){-d, -d, -d, -d};
            }
          } else if (__builtin_expect(__builtin_amdgcn_ballot_w64(__builtin_fmaxf(mx[0], mx[1]) > THR) != 0, 0)) {
#pragma unroll
            for (int t = 0; t < 2; ++t) {
              const float d = __builtin_fmaxf(red_max(mx[t]), 0.f);
              al[t] = __builtin_amdgcn_exp2f(-d);
              l[t] *= al[t];
              sm[0][t] -= d; sm[1][t] -= d;
              if (jm != 15) { sb[i & 3][0][t] -= d; sb[i & 3][1][t] -= d; negm[t] -= d; }
            }
          }
        }
      }
    };
    for (int u = 0; u < 8; ++u) {
      STAMP(2 + u);
#pragma unroll
      for (int i = 0; i < 16; ++i) {
        const Dma db = stK(16 * u + i + 3);
        pstep<8, 3, AH>([&](int m, int h) { if (h == 0) fa[m] = ldsr128((kb ^ (64u * (m & 3))) + ((i & 3) * SLOT + 4096 * (m >> 2))); },
                        [&](int m, int t) { mmK((i + 15) & 15, m, t); }, [&](int m, int t) { mmK(i, m, t); },
                        [&](int k, int h) {
                          if (h == 1 && i == 0 && k < 3) {
                            qf[k + 1][0] = ldsr128(xb + 2048 * (k < 2 ? k + 1 : 4)); qf[k + 1][1] = ldsr128(xb + 2048 * (k < 2 ? k + 1 : 4) + 1024);
                          }
                          if (h == 1) dma_pos<8, 1>(db, k, (i & 1) == 0);
                          soft(i, k, h, true);
                        },
                        [&]() { if (i == 0) { qf[0][0] = ldsr128(xb); qf[0][1] = ldsr128(xb + 1024); } });
        waitbar<8>();
      }
    }
    STAMP(10);
#pragma unroll
    for (int m = 10; m < 16; ++m) mmK(15, m >> 1, m & 1);
#pragma unroll
    for (int k = 0; k < 16; ++k) soft(0, k >> 1, k & 1, true);
    bar();
#pragma unroll
    for (int k = 0; k < 16; ++k) soft(1, k >> 1, k & 1, false);
    bar();
    bar(); bar(); bar(); bar(); bar();
    bar();
  } else {
    const unsigned grp = blockIdx.x & ~3u;
    auto stP = [&](unsigned ln, f32x4 a0, f32x4 a1, int gi, int ct) {
      const unsigned so = ((grp + ((hg + gi) & 3)) * 3 + gi - 1) * 16384 + qh * 8192 + (ct >> 2) * 4096;
      __builtin_amdgcn_raw_buffer_store_b128(pack8(a0, a1), sP, ln * 16 + (ct & 3) * 1024, so, 16);
    };
    auto putF = [&](unsigned ln, f32x4 a, int n) { ldsw128(ldsb + (n >> 2) * REG + qh * SLOT + (n & 3) * 1024 + ln * 16, __builtin_bit_cast(u32x4, a)); };
    if (role == 2) {
      setup(true);
#pragma unroll
      for (int i = 0; i < 10; ++i) ldsw128(xb + 1024 * i, (u32x4){0, 0, 0, 0});
      GAS unsigned long long* ep = (GAS unsigned long long*)(ws + WS_EPOCH + (blockIdx.x >> 2) * 128);
      unsigned long long tag;
      u32x4 wm[16];
      {
        bf16x8 xf[16][2];
        prologue(xf, 2, [&]() {
          asm volatile("global_load_dwordx2 %0, %1, off sc1" : "=&v"(tag) : "v"(ep) : "memory");
#pragma unroll
          for (int i = 0; i < 16; ++i) {
            const unsigned so = (WARM == 1 || WARM == 5) ? ((8 * hg + head(0)) * 128 + 64 * qh + 4 * i) * 1024 + ((rot + hg) & 3) * 256 : OOR;
            asm volatile("buffer_load_dwordx4 %0, %1, %2, %3 offen" : "=&v"(wm[i]) : "v"(dO), "s"(sO), "s"(so) : "memory");
          }
        });
      }
      bar();
      dma_all(stV(3)); bar();
      dma_all(stV(4)); bar();
      dma_all(stV(5));
      asm volatile("s_waitcnt vmcnt(8) lgkmcnt(0)\n\ts_barrier"
                   : "+v"(tag), "+v"(wm[0]), "+v"(wm[1]), "+v"(wm[2]), "+v"(wm[3]), "+v"(wm[4]), "+v"(wm[5]), "+v"(wm[6]), "+v"(wm[7]), "+v"(wm[8]), "+v"(wm[9]),
                     "+v"(wm[10]), "+v"(wm[11]), "+v"(wm[12]), "+v"(wm[13]), "+v"(wm[14]), "+v"(wm[15])::"memory");
      tag = ((unsigned long long)rfl((unsigned)(tag >> 32)) << 32) | rfl((unsigned)tag);
      tag = (tag ^ (tag >> 31)) * 0x9E3779B97F4A7C15ull + 0x632BE59BD9B4E019ull;
      unsigned pb = ldsb + PB + qh * 3072 + lane * 16;
      PIN(pb);
      f32x4 o[8][2], oa[4][2][2];
      bf16x8 pf[2][2], of[2][2];
#pragma unroll
      for (int i = 0; i < 16; ++i) { o[i >> 1][i & 1] = zero4(); oa[i >> 2][(i >> 1) & 1][i & 1] = zero4(); }
      zfa();
      pf[0][0] = pf[0][1] = pf[1][0] = pf[1][1] = of[0][0] = of[0][1] = of[1][0] = of[1][1] = zero8();
      f32x2 linv = {0.f, 0.f};
      auto mm3 = [&](int j, int m, int t) {
        if (m < 8) o[m][t] = mfma(FA(m), pf[j & 1][t], j == 0 ? zero4() : o[m][t]);
        else { const int iw = (j + 15) & 15; oa[iw & 3][m & 1][t] = mfma(FA(m), of[(iw >> 2) & 1][t], oa[iw & 3][m & 1][t]); }
      };
      auto packO = [&](int k) { return pack8(o[k & 6][k & 1] * linv[k & 1], o[(k & 6) + 1][k & 1] * linv[k & 1]); };
      for (int u = 0; u < 8; ++u) {
#pragma unroll
        for (int j = 0; j < 16; ++j) {
          const int iw = (j + 15) & 15;
          const Dma da = stV(16 * u + 6 + j);
          f32x2 alpha = {1.f, 1.f};
          pstep<10, D3, AH>([&](int m, int h) { if (m < 8) rdT(m, h, tbh, ((j + 3) & 3) * SLOT, m); else rdT(m, h, tbh, REG + ((j + 3) & 3) * SLOT, m - 2); },
                            [&](int m, int t) { mm3((j + 15) & 15, m, t); }, [&](int m, int t) { mm3(j, m, t); },
                            [&](int k, int h) {
                              if (h == 0) { dma_pos<10, 1>(da, k, (j & 1) == 1); return; }
                              if (j == 0 && k < 8) ldsw128(xb + 1024 * k, packO(k));
                              if (j > 0 && k == D3 - 1) {
                                if (__builtin_expect(__builtin_amdgcn_ballot_w64(alpha[0] != 1.f || alpha[1] != 1.f) != 0, 0)) {
#pragma unroll
                                  for (int ct = 0; ct < 8; ++ct) { o[ct][0] *= alpha[0]; o[ct][1] *= alpha[1]; }
                                }
                              }
                              if (j == 15 && k == 9) linv = *(const LAS f32x2*)(size_t)(pb + 6144 + 2056);
                              if (k == 8 && (iw & 3) == 0) { of[(iw >> 2) & 1][0] = ldsr128(xb + 2048 * (iw >> 2)); of[(iw >> 2) & 1][1] = ldsr128(xb + 2048 * (iw >> 2) + 1024); }
                            },
                            [&]() {
                              if (j > 0) alpha = *(const LAS f32x2*)(size_t)(pb + (j & 1) * 6144 + 2048);
                              pf[j & 1][0] = ldsr128(pb + (j & 1) * 6144); pf[j & 1][1] = ldsr128(pb + (j & 1) * 6144 + 1024);
                            });
          waitbar<8>();
        }
      }
#pragma unroll
      for (int m = 10 - D3; m < 10; ++m) { mm3(15, m, 0); mm3(15, m, 1); }
      rdT(8, 0, tbh, REG + 3 * SLOT, 6); rdT(8, 1, tbh, REG + 3 * SLOT, 6); rdT(9, 0, tbh, REG + 3 * SLOT, 7); rdT(9, 1, tbh, REG + 3 * SLOT, 7);
      bf16x8 og[8];
#pragma unroll
      for (int k = 0; k < 8; ++k) { const u32x4 w = packO(k); ldsw128(xb + 1024 * k, w); og[k] = __builtin_bit_cast(bf16x8, w); }
      mm3(0, 8, 0); mm3(0, 8, 1); mm3(0, 9, 0); mm3(0, 9, 1);
      bar();
      unsigned ln;
      asm volatile("v_mbcnt_lo_u32_b32 %0, -1, 0\n\tv_mbcnt_hi_u32_b32 %0, -1, %0" : "=v"(ln));
      const unsigned tl2 = tbh - 2 * REG;
#pragma unroll
      for (int j = 0; j < 4; ++j) {
        const int gi = (j + 1) & 3;
#pragma unroll
        for (int m = 0; m < 16; ++m) rdT(m >> 1, m & 1, m < 8 ? tl2 : tbh, ((m >> 2) & 1) * REG + j * SLOT, 6 + ((m >> 1) & 1));
#pragma unroll
        for (int m = 0; m < 8; ++m) {
          oa[gi][m & 1][0] = mfma(FA(m), og[m & 6], oa[gi][m & 1][0]);
          oa[gi][m & 1][1] = mfma(FA(m), og[(m & 6) + 1], oa[gi][m & 1][1]);
        }
        if (j < 3) {
          stP(ln, oa[gi][0][0], oa[gi][0][1], gi, 6); stP(ln, oa[gi][1][0], oa[gi][1][1], gi, 7);
          bar();
        }
      }
#pragma unroll
      for (int q = 0; q < 4; ++q) putF(ln, oa[0][q >> 1][q & 1], 12 + q);
      waitbar<0>();
      if (qh == 0) {
        if (ln < 3)
          __hip_atomic_store((GAS unsigned long long*)(ws + WS_FLAG + ((grp + ((hg + ln + 1) & 3)) * 4 + ln) * 128), tag, __ATOMIC_RELAXED, __HIP_MEMORY_SCOPE_AGENT);
        GAS unsigned long long* fl = (GAS unsigned long long*)(ws + WS_FLAG + (blockIdx.x * 4 + (ln < 3 ? ln : 0)) * 128);
        for (unsigned spins = 0; spins < (1u << 22); ++spins) {
          const unsigned long long v = __hip_atomic_load(fl, __ATOMIC_RELAXED, __HIP_MEMORY_SCOPE_AGENT);
          if (__all(v == tag)) break;
          __builtin_amdgcn_s_sleep(1);
        }
        if (hg == 0 && ln == 0) __hip_atomic_store(ep, tag, __ATOMIC_RELAXED, __HIP_MEMORY_SCOPE_AGENT);
      }
    } else {
      setup(true);
      { bf16x8 xf[16][2]; prologue(xf, 3, noextra); }
      dma_all(stO(2)); waitbar<8>();
      for (int s = 0; s < 17; ++s) waitbar<0>();
      dma_all(stO(20)); bar(); dma_all(stO(21)); bar(); dma_all(stO(22)); waitbar<8>();
      f32x4 oa[4][6][2];
      bf16x8 of[2][2];
#pragma unroll
      for (int i = 0; i < 48; ++i) oa[i / 12][(i % 12) >> 1][i & 1] = zero4();
      zfa();
      of[0][0] = of[0][1] = of[1][0] = of[1][1] = zero8();
      auto mm4 = [&](int nq, int par, int m, int t) { oa[nq][m][t] = mfma(FA(m), of[par][t], oa[nq][m][t]); };
      for (int u = 0; u < 7; ++u) {
#pragma unroll
        for (int i = 0; i < 16; ++i) {
          const int ip = (i + 15) & 15;
          const Dma db = stO(16 * u + 23 + i);
          pstep<6, D4, AH>([&](int m, int h) { rdT(m, h, tbh, REG + (i & 3) * SLOT, m); },
                           [&](int m, int t) { mm4(ip & 3, (ip >> 2) & 1, m, t); }, [&](int m, int t) { mm4(i & 3, (i >> 2) & 1, m, t); },
                           [&](int k, int h) { if (h == 0) dma_pos<6, 0>(db, k, (i & 1) == 1); },
                           [&]() { if ((i & 3) == 0) { of[(i >> 2) & 1][0] = ldsr128(xb + 2048 * (i >> 2)); of[(i >> 2) & 1][1] = ldsr128(xb + 2048 * (i >> 2) + 1024); } });
          waitbar<8>();
        }
      }
      STAMP(11);
      unsigned ln;
      asm volatile("v_mbcnt_lo_u32_b32 %0, -1, 0\n\tv_mbcnt_hi_u32_b32 %0, -1, %0" : "=v"(ln));
      const unsigned tl2 = tbh - 2 * REG;
#pragma unroll
      for (int j = 0; j < 4; ++j) {
        const int gi = (j + 1) & 3;
#pragma unroll
        for (int q = 0; q < 4; ++q) {
          pstep<6, D4, AH>([&](int m, int h) { rdT(m, h, q < 2 ? tl2 : tbh, (q & 1) * REG + j * SLOT, m); },
                           [&](int m, int t) { if (q > 0) mm4(gi, (q - 1) & 1, m, t); else mm4(j == 0 ? 3 : j, 1, m, t); },
                           [&](int m, int t) { mm4(gi, q & 1, m, t); },
                           [&](int k, int h) {
                             if (h == 1 && j > 0 && q == 0) stP(ln, oa[j][k][0], oa[j][k][1], j, k);
                           },
                           [&]() { of[q & 1][0] = ldsr128(xb + 2048 * q); of[q & 1][1] = ldsr128(xb + 2048 * q + 1024); });
          if (j == 3 && q == DRQ) waitbar<0>();
        }
        if (j < 3) bar();
      }
#pragma unroll
      for (int m = 6 - D4; m < 6; ++m) { mm4(0, 1, m, 0); mm4(0, 1, m, 1); }
#pragma unroll
      for (int n = 0; n < 12; ++n) putF(ln, oa[0][n >> 1][n & 1], n);
      STAMP(12);
    }
  }
  waitbar<0>();
  {
    unsigned ln;
    asm volatile("v_mbcnt_lo_u32_b32 %0, -1, 0\n\tv_mbcnt_hi_u32_b32 %0, -1, %0" : "=v"(ln));
    f32x4 a[4];
    u32x4 in[6];
#pragma unroll
    for (int j = 0; j < 3; ++j)
#pragma unroll
      for (int c = 0; c < 2; ++c)
        in[2 * j + c] = __builtin_amdgcn_raw_buffer_load_b128(sP, ln * 16 + c * 1024, (blockIdx.x * 3 + j) * 16384 + qh * 8192 + role * 2048, 16);
#pragma unroll
    for (int q = 0; q < 4; ++q) {
      a[q] = __builtin_bit_cast(f32x4, ldsr128(ldsb + role * REG + qh * SLOT + q * 1024 + ln * 16));
#pragma unroll
      for (int j = 0; j < 3; ++j) {
        const unsigned w0 = in[2 * j + (q >> 1)][2 * (q & 1)], w1 = in[2 * j + (q >> 1)][2 * (q & 1) + 1];
        a[q] += (f32x4){__uint_as_float(w0 << 16), __uint_as_float(w0 & 0xffff0000u), __uint_as_float(w1 << 16), __uint_as_float(w1 & 0xffff0000u)};
      }
    }
    char* orow = out + ((size_t)b * 64 + 32 * qh + (ln & 15)) * 2048 + p * 1024 + ((rot + hg) & 3) * 256 + role * 64 + ((ln & 16) * 2 + (ln >> 5) * 16);
    *(u32x4*)(orow) = swap16(pack8(a[0], a[2]));
    *(u32x4*)(orow + 16 * 2048) = swap16(pack8(a[1], a[3]));
  }
}

extern "C" void kernel_launch(void* const* d_in, const int* in_sizes, int n_in, void* d_out, int out_size, void* d_ws, size_t ws_size,
                              hipStream_t stream) {
  (void)in_sizes; (void)n_in; (void)out_size; (void)ws_size;
  (void)hipFuncSetAttribute((const void*)mha_fused, hipFuncAttributeMaxDynamicSharedMemorySize, LDS_BYTES);
  mha_fused<<<256, 512, LDS_BYTES, stream>>>((const char*)d_in[0], (const char*)d_in[1], (const char*)d_in[2], (const char*)d_in[3],
                                             (const char*)d_in[4], (const char*)d_in[5], (const char*)d_in[6], (const char*)d_in[7],
                                             (const char*)d_in[8], (const char*)d_in[9], (char*)d_ws, (char*)d_out);
}
```

```cpp
#include <hip/hip_runtime.h>
#include <hip/hip_bf16.h>

typedef __amdgpu_buffer_rsrc_t rsrc_t;
using bf16x8 = __attribute__((ext_vector_type(8))) short;
using s16x4 = __attribute__((ext_vector_type(4))) short;
using f32x4 = __attribute__((ext_vector_type(4))) float;
using f32x2 = __attribute__((ext_vector_type(2))) float;
using u32x4 = __attribute__((ext_vector_type(4))) unsigned;
using bf16x2_t = __attribute__((ext_vector_type(2))) __bf16;
typedef short v4i16_t __attribute__((ext_vector_type(4)));
#define LAS __attribute__((address_space(3)))
#define GAS __attribute__((address_space(1)))
#define DI __device__ __forceinline__
#define SBAR() __builtin_amdgcn_sched_barrier(0)
#define PIN(x) asm volatile("" : "+v"(x))

constexpr int SLOT = 8192, REG = 32768;
constexpr int XB = 4 * REG;
constexpr int PB = XB + 2 * 10240;
constexpr int LDS_BYTES = PB + 4 * 3072;
constexpr unsigned WS_FLAG = 32u << 20, WS_EPOCH = 33u << 20;
constexpr float SC = 0.08838834764831845f * 1.4426950408889634f;
constexpr unsigned OOR = 0x80000000u;
constexpr unsigned NOLDS = 0x30000u;
#define THR 24.0f
#define ABL_KV 1
#define ABL_W 1
#define KVNT true
#define PBF16 1
#define DRQ 1
#define WARM 1
#define WS0 2
#define ORD 0
#define ROLEMAP 0x1032
#define D1 3
#define D3 3
#define D4 2
#define AH 2

DI unsigned rfl(unsigned x) { return __builtin_amdgcn_readfirstlane(x); }
DI unsigned cvtpk(float lo, float hi) { f32x2 v = {lo, hi}; return __builtin_bit_cast(unsigned, __builtin_convertvector(v, bf16x2_t)); }
DI f32x4 mfma(bf16x8 a, bf16x8 b, f32x4 c) { return __builtin_amdgcn_mfma_f32_16x16x32_bf16(a, b, c, 0, 0, 0); }
DI s16x4 trd(unsigned addr) { return __builtin_bit_cast(s16x4, __builtin_amdgcn_ds_read_tr16_b64_v4i16((LAS v4i16_t*)(size_t)addr)); }
DI bf16x8 ldsr128(unsigned addr) { return *(const LAS bf16x8*)(size_t)addr; }
DI void ldsw128(unsigned addr, u32x4 v) { *(LAS u32x4*)(size_t)addr = v; }
DI bf16x8 cat(s16x4 a, s16x4 b) { return (bf16x8){a[0], a[1], a[2], a[3], b[0], b[1], b[2], b[3]}; }
DI f32x4 zero4() { return (f32x4){0.f, 0.f, 0.f, 0.f}; }
DI bf16x8 zero8() { return (bf16x8){0, 0, 0, 0, 0, 0, 0, 0}; }
DI u32x4 pack8(f32x4 a, f32x4 b) { return (u32x4){cvtpk(a[0], a[1]), cvtpk(a[2], a[3]), cvtpk(b[0], b[1]), cvtpk(b[2], b[3])}; }
DI u32x4 swap16(u32x4 w) {
  auto s0 = __builtin_amdgcn_permlane16_swap(w[0], w[2], false, false);
  auto s1 = __builtin_amdgcn_permlane16_swap(w[1], w[3], false, false);
  return (u32x4){s0[0], s1[0], s0[1], s1[1]};
}
DI float max3f(float a, float b, float c) { return __builtin_fmaxf(__builtin_fmaxf(a, b), c); }
DI float red_max(float v) {
  auto r = __builtin_amdgcn_permlane16_swap(__float_as_uint(v), __float_as_uint(v), false, false);
  v = __builtin_fmaxf(__uint_as_float(r[0]), __uint_as_float(r[1]));
  auto q = __builtin_amdgcn_permlane32_swap(__float_as_uint(v), __float_as_uint(v), false, false);
  return __builtin_fmaxf(__uint_as_float(q[0]), __uint_as_float(q[1]));
}
DI float red_sum(float v) {
  auto r = __builtin_amdgcn_permlane16_swap(__float_as_uint(v), __float_as_uint(v), false, false);
  v = __uint_as_float(r[0]) + __uint_as_float(r[1]);
  auto q = __builtin_amdgcn_permlane32_swap(__float_as_uint(v), __float_as_uint(v), false, false);
  return __uint_as_float(q[0]) + __uint_as_float(q[1]);
}
template <int N> DI void waitbar() { asm volatile("s_waitcnt vmcnt(%0) lgkmcnt(0)\n\ts_barrier" ::"i"(N) : "memory"); SBAR(); }
DI void bar() { asm volatile("s_waitcnt lgkmcnt(0)\n\ts_barrier" ::: "memory"); SBAR(); }

struct Dma { rsrc_t srd; unsigned m0, so, sstep, v0; bool nt; int kind; };
template <int J> DI void dma_piece(const Dma& d) {
  const unsigned v = d.v0 ^ (d.kind == 1 ? 64u * J : d.kind == 0 ? 128u * (J & 1) : 128u * (J >> 1));
  const unsigned so = d.so + J * d.sstep;
  if (J == 0) {
    if (d.nt) asm volatile("s_mov_b32 m0, %0\n\ts_nop 0\n\tbuffer_load_dwordx4 %1, %2, %3 offen offset:%4 nt lds" ::"s"(d.m0), "v"(v), "s"(d.srd), "s"(so), "i"(1024 * J) : "m0");
    else asm volatile("s_mov_b32 m0, %0\n\ts_nop 0\n\tbuffer_load_dwordx4 %1, %2, %3 offen offset:%4 lds" ::"s"(d.m0), "v"(v), "s"(d.srd), "s"(so), "i"(1024 * J) : "m0");
  } else {
    if (d.nt) asm volatile("buffer_load_dwordx4 %0, %1, %2 offen offset:%3 nt lds" ::"v"(v), "s"(d.srd), "s"(so), "i"(1024 * J));
    else asm volatile("buffer_load_dwordx4 %0, %1, %2 offen offset:%3 lds" ::"v"(v), "s"(d.srd), "s"(so), "i"(1024 * J));
  }
}
DI void dma_all(const Dma& d) { dma_piece<0>(d); dma_piece<1>(d); dma_piece<2>(d); dma_piece<3>(d); }
template <int K0, int KS> DI void dma_slot(const Dma& d, int k) {
  if (k == K0) dma_piece<0>(d);
  if (k == K0 + KS) dma_piece<1>(d);
  if (k == K0 + 2 * KS) dma_piece<2>(d);
  if (k == K0 + 3 * KS) dma_piece<3>(d);
}
template <int N, int PH> DI void dma_pos(const Dma& d, int k, bool early) {
  if (ORD == 0) { if (N >= 8) dma_slot<PH, 2>(d, k); else dma_slot<0, 1>(d, k); }
  else { if (early) dma_slot<0, 1>(d, k); else dma_slot<N - 4, 1>(d, k); }
}

struct NoPre { DI void operator()() const {} };
template <int N, int D, int A, class RD, class MP, class MC, class FL, class PR = NoPre>
DI void pstep(RD rd, MP mprev, MC mcur, FL fill, PR pre = PR()) {
#pragma unroll
  for (int k = 0; k < N; ++k) {
#pragma unroll
    for (int h = 0; h < 2; ++h) {
      if (k < D) mprev(N - D + k, h); else mcur(k - D, h);
      SBAR();
      if (k == 0) {
        if (h == 0) pre();
#pragma unroll
        for (int q = h; q < A; q += 2) { rd(q, 0); rd(q, 1); }
      }
      if (k + A < N) rd(k + A, h);
      fill(k, h);
      SBAR();
    }
  }
}

#define STAMP(i)

__global__ __launch_bounds__(512, 2) void mha_fused(const char* __restrict__ xA, const char* __restrict__ kA, const char* __restrict__ vA,
                                                    const char* __restrict__ wqA, const char* __restrict__ woA, const char* __restrict__ xB,
                                                    const char* __restrict__ kB, const char* __restrict__ vB, const char* __restrict__ wqB,
                                                    const char* __restrict__ woB, char* __restrict__ ws, char* __restrict__ out) {
  extern __shared__ __attribute__((aligned(16))) char lds[];
  unsigned lane = threadIdx.x & 63;
  const unsigned tid = threadIdx.x, wid = rfl(tid >> 6), role = (ROLEMAP >> (4 * (wid >> 1))) & 3, qh = wid & 1;
  const unsigned xg = blockIdx.x & 7, p = xg >> 2, hg = xg & 3, b = blockIdx.x >> 3;
  const unsigned rot = b >> 3;
  const char* kc = (p ? kB : kA) + (size_t)(b * 32 + hg * 8) * 131072;
  const char* vc = (p ? vB : vA) + (size_t)(b * 32 + hg * 8) * 131072;
  const char* wq = p ? wqB : wqA;
  const char* wo = p ? woB : woA;
  const char* xp = (p ? xB : xA) + (size_t)b * 65536;
  const rsrc_t sK = __builtin_amdgcn_make_buffer_rsrc((void*)kc, (short)0, ABL_KV << 20, 0x00020000);
  const rsrc_t sV = __builtin_amdgcn_make_buffer_rsrc((void*)vc, (short)0, ABL_KV << 20, 0x00020000);
  const rsrc_t sQ = __builtin_amdgcn_make_buffer_rsrc((void*)wq, (short)0, ABL_W << 22, 0x00020000);
  const rsrc_t sO = __builtin_amdgcn_make_buffer_rsrc((void*)wo, (short)0, ABL_W << 22, 0x00020000);
  const rsrc_t sX = __builtin_amdgcn_make_buffer_rsrc((void*)xp, (short)0, 65536, 0x00020000);
  const unsigned ldsb = (unsigned)(size_t)lds;
  unsigned g, r, dq, dO, dV, dK, tq, tbh, xb;
  auto setup = [&](bool opaque) {
    if (opaque) asm volatile("v_mbcnt_lo_u32_b32 %0, -1, 0\n\tv_mbcnt_hi_u32_b32 %0, -1, %0" : "=v"(lane));
    g = lane >> 4; r = lane & 15;
    const unsigned swT = 16 * (r ^ (2 * g));
    dq = g * 8192 + swT; dO = g * 1024 + swT; dV = g * 256 + swT; dK = g * 256 + 16 * (r ^ g);
    const unsigned q4 = r >> 2;
    tq = ldsb + (8 * g + q4) * 256 + 32 * (q4 | ((g & 1) << 2)) + 8 * (lane & 3);
    tbh = ldsb + 2 * REG + (lane >> 2) * 256 + 32 * ((lane >> 2) & 7) + 8 * (lane & 3);
    xb = ldsb + XB + qh * 10240 + lane * 16;
    if (opaque) { PIN(tbh); PIN(xb); }
  };
  setup(false);
  STAMP(0);
#pragma unroll
  for (int i = 0; i < 8; ++i) {
    const unsigned m0 = ldsb + role * REG + (1 + qh) * SLOT + i * 1024, so = (16 * role + 8 * qh + i) * 1024;
    const unsigned v = 16 * (((lane ^ (8 * qh + i)) + 16 * rot) & 63);
    asm volatile("s_mov_b32 m0, %0\n\ts_nop 0\n\tbuffer_load_dwordx4 %1, %2, %3 offen lds" ::"s"(m0), "v"(v), "s"(sX), "s"(so) : "m0");
  }
  const unsigned mq = ldsb + qh * 4096;
  auto head = [&](unsigned u) -> unsigned { return (u + b) & 7u; };
  auto mkWq = [&](unsigned u, unsigned i, unsigned reg, unsigned slot, bool nt = false) -> Dma {
    return Dma{sQ, mq + reg * REG + slot * SLOT, (32 * ((i + 4 * rot) & 15) + 16 * qh) * 8192 + (8 * hg + head(u)) * 256, 4 * 8192 - 1024, dq, nt, 2};
  };
  auto mkWo = [&](unsigned u, unsigned dblk, unsigned nq, unsigned reg, unsigned slot) -> Dma {
    return Dma{sO, mq + reg * REG + slot * SLOT, ((8 * hg + head(u)) * 128 + 32 * dblk + 16 * qh) * 1024 + ((nq + rot + hg) & 3) * 256, 4 * 1024 - 1024, dO, false, 0};
  };
  auto stK = [&](int sg) -> Dma {
    const bool ok = (unsigned)sg < 128u;
    return Dma{sK, ok ? mq + REG + (sg & 3) * SLOT : NOLDS, ok ? head(sg >> 4) * 131072 + (sg & 15) * 8192 + qh * 4096 : OOR, 0, dK, KVNT, 1};
  };
  auto stV = [&](int sg) -> Dma {
    const int t = sg - 3; const bool ok = (unsigned)t < 128u;
    return Dma{sV, ok ? mq + 2 * REG + (sg & 3) * SLOT : NOLDS, ok ? head(t >> 4) * 131072 + (t & 15) * 8192 + qh * 4096 : OOR, 0, dV, KVNT, 0};
  };
  auto stQ = [&](int sg) -> Dma {
    const int t = sg + 17; const bool ok = t < 128;
    Dma d = mkWq(t >> 4, t & 15, 0, sg & 3);
    d.m0 = ok ? d.m0 : NOLDS; d.so = ok ? d.so : OOR;
    return d;
  };
  auto stO = [&](int sg) -> Dma {
    const int t = sg - 20;
    Dma d = mkWo(t >> 4, (t >> 2) & 3, t & 3, 3, sg & 3);
    d.m0 = t < 112 ? d.m0 : NOLDS; d.so = (unsigned)t < 112u ? d.so : OOR;
    return d;
  };
  { Dma w = mkWo(0, role, 0, role, 0); if (WARM < 4) { w.so = OOR; w.m0 = NOLDS; } w.nt = WARM == 5; dma_all(w); }
  asm volatile("s_barrier" ::: "memory");
  dma_all(mkWq(0, role, role, 3, true));
  s16x4 fh[10][2];
  auto FA = [&](int f) { return cat(fh[f][0], fh[f][1]); };
  auto rdT = [&](int f, int h, unsigned tb, unsigned off, int ct) { fh[f][h] = trd((tb ^ (32u * ct)) + (off + 4096 * h)); };
  auto rdQ = [&](int f, int h, unsigned tb, unsigned off, int ct) { fh[f][h] = trd((tb ^ (32u * ct)) + (off + 1024 * h)); };
  auto nop = [&](int, int) {};
  auto zfa = [&]() {
#pragma unroll
    for (int i = 0; i < 10; ++i) fh[i][0] = fh[i][1] = (s16x4){0, 0, 0, 0};
  };
  auto prologue = [&](bf16x8 (*xf)[2], const int R, auto extra) {
    waitbar<8>();
    {
      const Dma f4 = mkWq(0, 4 + R, R, 0);
      const unsigned xa = ldsb + 2 * qh * REG + (1 + (r >> 3)) * SLOT + (r & 7) * 1024 + 16 * (g ^ r);
#pragma unroll
      for (int i = 0; i < 16; ++i) {
        xf[i][0] = ldsr128((xa ^ (64u * (i & 3))) + 256 * (i >> 2));
        xf[i][1] = ldsr128((xa ^ (64u * (i & 3))) + 256 * (i >> 2) + REG);
        dma_slot<3, 4>(f4, i);
      }
    }
    waitbar<4>();
    if (R == 0) STAMP(1);
    f32x4 q0[2][2] = {{zero4(), zero4()}, {zero4(), zero4()}};
    unsigned tqh = tq + 2 * REG;
    PIN(tqh);
    auto mm0 = [&](int j, int m, int t) { q0[m & 1][t] = mfma(FA(m), xf[4 * j + (m >> 1)][t], q0[m & 1][t]); };
#pragma unroll
    for (int j = 0; j < 4; ++j) {
      if (j == 0) { dma_all(mkWq(0, 8 + R, R, 1)); dma_all(mkWq(0, 12 + R, R, 2)); }
      if (j >= 1) { const int sg = j - 2; dma_all(R == 0 ? stQ(sg) : R == 1 ? stK(sg) : R == 2 ? stV(sg) : stO(sg)); }
      pstep<8, 3, 2>([&](int m, int h) { rdQ(m, h, m < 4 ? tq : tqh, ((m >> 1) & 1) * REG + ((j + 3) & 3) * SLOT, 2 * R + (m & 1)); },
                     [&](int m, int t) { if (j > 0) mm0(j - 1, m, t); }, [&](int m, int t) { mm0(j, m, t); }, nop);
      if (j == 0) extra();
      if (R == 2) { if (j < 3) waitbar<25>(); else bar(); } else waitbar<8>();
    }
#pragma unroll
    for (int m = 10; m < 16; ++m) mm0(3, m >> 1, m & 1);
    ldsw128(xb + 2048 * R + (R == 3 ? 2048 : 0), swap16(pack8(q0[0][0] * SC, q0[1][0] * SC)));
    ldsw128(xb + 2048 * R + (R == 3 ? 2048 : 0) + 1024, swap16(pack8(q0[0][1] * SC, q0[1][1] * SC)));
    SBAR();
  };
  auto noextra = [&]() {};
  const rsrc_t sP = __builtin_amdgcn_make_buffer_rsrc((void*)ws, (short)0, WS_FLAG, 0x00020000);

  if (role == 0) {
    setup(true);
    bf16x8 xf[16][2];
    prologue(xf, 0, noextra);
    f32x4 qacc[8][2];
#pragma unroll
    for (int i = 0; i < 8; ++i) qacc[i][0] = qacc[i][1] = zero4();
    auto mmQ = [&](int i, int m, int t) { qacc[m][t] = mfma(FA(m), xf[i][t], i == 0 ? zero4() : qacc[m][t]); };
    auto putQ = [&](int k) {
      ldsw128(xb + 1024 * (k < 6 ? k : k + 2), swap16(pack8(qacc[k & 6][k & 1] * SC, qacc[(k & 6) + 1][k & 1] * SC)));
    };
    for (int u = 1; u < 8; ++u) {
#pragma unroll
      for (int i = 0; i < 16; ++i) {
        const Dma da = stQ(16 * (u - 1) + i + 2);
        pstep<8, D1, AH>([&](int m, int h) { rdQ(m, h, tq, ((i + 3) & 3) * SLOT, m); },
                         [&](int m, int t) { mmQ((i + 15) & 15, m, t); }, [&](int m, int t) { mmQ(i, m, t); },
                         [&](int k, int h) { if (h == 0) dma_pos<8, 0>(da, k, (i & 1) == 0); if (h == 1 && i == 0 && u > 1) putQ(k); });
        waitbar<8>();
      }
    }
#pragma unroll
    for (int m = 8 - D1; m < 8; ++m) { mmQ(15, m, 0); mmQ(15, m, 1); }
#pragma unroll
    for (int k = 0; k < 8; ++k) putQ(k);
    bar();
#pragma unroll
    for (int j = 0; j < 4; ++j) { dma_all(mkWo(7, 0, j + 1, 0, j)); bar(); bar(); bar(); bar(); }
    dma_all(mkWo(7, 1, 1, 1, 0)); dma_all(mkWo(7, 1, 2, 1, 1)); bar();
    dma_all(mkWo(7, 2, 1, 2, 0)); dma_all(mkWo(7, 3, 1, 3, 0)); dma_all(mkWo(7, 1, 3, 1, 2)); bar();
    dma_all(mkWo(7, 2, 2, 2, 1)); dma_all(mkWo(7, 3, 2, 3, 1)); dma_all(mkWo(7, 1, 0, 1, 3)); bar();
    dma_all(mkWo(7, 2, 3, 2, 2)); dma_all(mkWo(7, 3, 3, 3, 2)); dma_all(mkWo(7, 2, 0, 2, 3)); waitbar<28>();
    dma_all(mkWo(7, 3, 0, 3, 3)); waitbar<20>();
    waitbar<8>();
    waitbar<0>();
    bar();
  } else if (role == 1) {
    setup(true);
    { bf16x8 xf[16][2]; prologue(xf, 1, noextra); }
    dma_all(stK(2)); waitbar<8>();
    const unsigned sig = ((g & 1) << 1) | (g >> 1);
    const unsigned kb = ldsb + REG + r * 256 + 16 * (sig ^ r);
    unsigned pb = ldsb + PB + qh * 3072 + lane * 16;
    PIN(pb);
    f32x4 sb[4][2][2], negm[2] = {zero4(), zero4()};
    bf16x8 qf[4][2], fa[8];
#pragma unroll
    for (int i = 0; i < 16; ++i) sb[i >> 2][(i >> 1) & 1][i & 1] = zero4();
#pragma unroll
    for (int i = 0; i < 8; ++i) { qf[i >> 1][i & 1] = zero8(); fa[i] = zero8(); }
    float mx[2] = {0.f, 0.f}, mc = 0.f;
    unsigned flag = 0;
    auto mmK = [&](int i, int m, int t) {
      sb[i & 3][m >> 2][t] = mfma(fa[m], qf[m & 3][t], (m & 3) == 0 ? (i < 2 ? zero4() : negm[t]) : sb[i & 3][m >> 2][t]);
    };
    auto soft = [&](int i, int k, int h, bool chk) {
      const int je = (i + 14) & 15, jm = (i + 15) & 15;
      auto& se = sb[je & 3];
      auto& sm = sb[jm & 3];
      auto g0 = [](int q) { return q < 4 ? 3 * q : 12 + 2 * (q - 4); };
      auto gn = [](int q) { return q < 4 ? 3 : 2; };
      if (h == 0 && k < 6) {
#pragma unroll
        for (int e = g0(k); e < g0(k) + gn(k); ++e) se[(e >> 2) & 1][e >> 3][e & 3] = __builtin_amdgcn_exp2f(se[(e >> 2) & 1][e >> 3][e & 3]);
      }
      if (h == 0) return;
      if (k == 3) { u32x4 w = pack8(se[0][0], se[1][0]); w[0] |= flag; ldsw128(pb + (je & 1) * 6144, w); }
      if (k == 6) ldsw128(pb + (je & 1) * 6144 + 1024, pack8(se[0][1], se[1][1]));
      if (chk) {
        if (k == 3) { mx[0] = max3f(sm[0][0][0], sm[0][0][1], sm[0][0][2]); mx[1] = max3f(sm[0][1][0], sm[0][1][1], sm[0][1][2]); PIN(mx[0]); PIN(mx[1]); }
        if (k == 4) { mx[0] = max3f(mx[0], sm[0][0][3], sm[1][0][0]); mx[1] = max3f(mx[1], sm[0][1][3], sm[1][1][0]); PIN(mx[0]); PIN(mx[1]); }
        if (k == 5) { mx[0] = max3f(mx[0], sm[1][0][1], sm[1][0][2]); mx[1] = max3f(mx[1], sm[1][1][1], sm[1][1][2]); PIN(mx[0]); PIN(mx[1]); }
        if (k == 6 && jm != 0) { mc = max3f(sm[1][0][3], sm[1][1][3], mx[0]); PIN(mc); mc = max3f(mc, mx[1], 0.f); }
        if (k == 7) {
          flag = 0;
          if (jm == 0) {
#pragma unroll
            for (int t = 0; t < 2; ++t) {
              const float d = red_max(__builtin_fmaxf(mx[t], sm[1][t][3]));
              sm[0][t] -= d; sm[1][t] -= d; sb[i & 3][0][t] -= d; sb[i & 3][1][t] -= d;
              negm[t] = (f32x4){-d, -d, -d, -d};
            }
          } else if (__builtin_expect(__builtin_amdgcn_ballot_w64(mc > THR) != 0, 0)) {
            flag = 0x80000000u;
            f32x2 al;
#pragma unroll
            for (int t = 0; t < 2; ++t) {
              const float d = __builtin_fmaxf(red_max(__builtin_fmaxf(mx[t], sm[1][t][3])), 0.f);
              al[t] = __builtin_amdgcn_exp2f(-d);
              sm[0][t] -= d; sm[1][t] -= d;
              if (jm != 15) { sb[i & 3][0][t] -= d; sb[i & 3][1][t] -= d; negm[t] -= d; }
            }
            *(LAS f32x2*)(size_t)(pb + (jm & 1) * 6144 + 2048 + 8 * ((jm >> 1) & 1)) = al;
          }
        }
      }
    };
    for (int u = 0; u < 8; ++u) {
      STAMP(2 + u);
#pragma unroll
      for (int i = 0; i < 16; ++i) {
        const Dma db = stK(16 * u + i + 3);
        pstep<8, 3, AH>([&](int m, int h) { if (h == 0) fa[m] = ldsr128((kb ^ (64u * (m & 3))) + ((i & 3) * SLOT + 4096 * (m >> 2))); },
                        [&](int m, int t) { mmK((i + 15) & 15, m, t); }, [&](int m, int t) { mmK(i, m, t); },
                        [&](int k, int h) {
                          if (h == 1 && i == 0 && k < 3) {
                            qf[k + 1][0] = ldsr128(xb + 2048 * (k < 2 ? k + 1 : 4)); qf[k + 1][1] = ldsr128(xb + 2048 * (k < 2 ? k + 1 : 4) + 1024);
                          }
                          if (h == 1) dma_pos<8, 1>(db, k, (i & 1) == 0);
                          soft(i, k, h, true);
                        },
                        [&]() { if (i == 0) { qf[0][0] = ldsr128(xb); qf[0][1] = ldsr128(xb + 1024); } });
        waitbar<8>();
      }
    }
    STAMP(10);
#pragma unroll
    for (int m = 10; m < 16; ++m) mmK(15, m >> 1, m & 1);
#pragma unroll
    for (int k = 0; k < 16; ++k) soft(0, k >> 1, k & 1, true);
    bar();
#pragma unroll
    for (int k = 0; k < 16; ++k) soft(1, k >> 1, k & 1, false);
    bar();
    bar(); bar(); bar(); bar(); bar();
    bar();
  } else {
    const unsigned grp = blockIdx.x & ~3u;
    auto stP = [&](unsigned ln, f32x4 a0, f32x4 a1, int gi, int ct) {
      const unsigned so = ((grp + ((hg + gi) & 3)) * 3 + gi - 1) * 16384 + qh * 8192 + (ct >> 2) * 4096;
      __builtin_amdgcn_raw_buffer_store_b128(pack8(a0, a1), sP, ln * 16 + (ct & 3) * 1024, so, 16);
    };
    auto putF = [&](unsigned ln, f32x4 a, int n) { ldsw128(ldsb + (n >> 2) * REG + qh * SLOT + (n & 3) * 1024 + ln * 16, __builtin_bit_cast(u32x4, a)); };
    if (role == 2) {
      setup(true);
#pragma unroll
      for (int i = 0; i < 10; ++i) ldsw128(xb + 1024 * i, (u32x4){0, 0, 0, 0});
      GAS unsigned long long* ep = (GAS unsigned long long*)(ws + WS_EPOCH + (blockIdx.x >> 2) * 128);
      unsigned long long tag;
      u32x4 wm[16];
      {
        bf16x8 xf[16][2];
        prologue(xf, 2, [&]() {
          asm volatile("global_load_dwordx2 %0, %1, off sc1" : "=&v"(tag) : "v"(ep) : "memory");
#pragma unroll
          for (int i = 0; i < 16; ++i) {
            const unsigned so = (WARM == 1 || WARM == 5) ? ((8 * hg + head(0)) * 128 + 64 * qh + 4 * i) * 1024 + ((rot + hg) & 3) * 256 : OOR;
            asm volatile("buffer_load_dwordx4 %0, %1, %2, %3 offen" : "=&v"(wm[i]) : "v"(dO), "s"(sO), "s"(so) : "memory");
          }
        });
      }
      bar();
      dma_all(stV(3)); bar();
      dma_all(stV(4)); bar();
      dma_all(stV(5));
      asm volatile("s_waitcnt vmcnt(8) lgkmcnt(0)\n\ts_barrier"
                   : "+v"(tag), "+v"(wm[0]), "+v"(wm[1]), "+v"(wm[2]), "+v"(wm[3]), "+v"(wm[4]), "+v"(wm[5]), "+v"(wm[6]), "+v"(wm[7]), "+v"(wm[8]), "+v"(wm[9]),
                     "+v"(wm[10]), "+v"(wm[11]), "+v"(wm[12]), "+v"(wm[13]), "+v"(wm[14]), "+v"(wm[15])::"memory");
      tag = ((unsigned long long)rfl((unsigned)(tag >> 32)) << 32) | rfl((unsigned)tag);
      tag = (tag ^ (tag >> 31)) * 0x9E3779B97F4A7C15ull + 0x632BE59BD9B4E019ull;
      unsigned pb = ldsb + PB + qh * 3072 + lane * 16;
      PIN(pb);
      f32x4 o[8][2], oa[4][2][2];
      bf16x8 pf[2][2], of[2][2];
#pragma unroll
      for (int i = 0; i < 16; ++i) { o[i >> 1][i & 1] = zero4(); oa[i >> 2][(i >> 1) & 1][i & 1] = zero4(); }
      zfa();
      pf[0][0] = pf[0][1] = pf[1][0] = pf[1][1] = of[0][0] = of[0][1] = of[1][0] = of[1][1] = zero8();
      f32x2 linv = {0.f, 0.f};
      f32x4 lacc[2] = {zero4(), zero4()};
      bf16x8 ones = {0x3f80, 0x3f80, 0x3f80, 0x3f80, 0x3f80, 0x3f80, 0x3f80, 0x3f80};
      PIN(ones);
      auto mm3 = [&](int j, int m, int t) {
        if (m < 8) o[m][t] = mfma(FA(m), pf[j & 1][t], j == 0 ? zero4() : o[m][t]);
        else { const int iw = (j + 15) & 15; oa[iw & 3][m & 1][t] = mfma(FA(m), of[(iw >> 2) & 1][t], oa[iw & 3][m & 1][t]); }
      };
      auto packO = [&](int k) { return pack8(o[k & 6][k & 1] * linv[k & 1], o[(k & 6) + 1][k & 1] * linv[k & 1]); };
      for (int u = 0; u < 8; ++u) {
#pragma unroll
        for (int j = 0; j < 16; ++j) {
          const int iw = (j + 15) & 15;
          const Dma da = stV(16 * u + 6 + j);
          pstep<10, D3, AH>([&](int m, int h) { if (m < 8) rdT(m, h, tbh, ((j + 3) & 3) * SLOT, m); else rdT(m, h, tbh, REG + ((j + 3) & 3) * SLOT, m - 2); },
                            [&](int m, int t) { mm3((j + 15) & 15, m, t); }, [&](int m, int t) { mm3(j, m, t); },
                            [&](int k, int h) {
                              if (h == 0) { dma_pos<10, 1>(da, k, (j & 1) == 1); return; }
                              if (j == 0 && k < 8) ldsw128(xb + 1024 * k, packO(k));
                              if (j > 0 && k == D3 - 1) {
                                if (__builtin_expect(__builtin_amdgcn_ballot_w64((int)__builtin_bit_cast(u32x4, pf[j & 1][0])[0] < 0) != 0, 0)) {
                                  const f32x2 alpha = *(const LAS f32x2*)(size_t)(pb + (j & 1) * 6144 + 2048 + 8 * ((j >> 1) & 1));
                                  { u32x4 w = __builtin_bit_cast(u32x4, pf[j & 1][0]); w[0] &= 0x7fffffffu; pf[j & 1][0] = __builtin_bit_cast(bf16x8, w); }
                                  f32x4 a0 = {alpha[0], alpha[0], alpha[0], alpha[0]}, a1 = {alpha[1], alpha[1], alpha[1], alpha[1]};
                                  PIN(a0); PIN(a1);
#pragma unroll
                                  for (int ct = 0; ct < 8; ++ct) { o[ct][0] *= a0; o[ct][1] *= a1; }
                                  lacc[0] *= a0; lacc[1] *= a1;
                                }
                              }
                              if (k == 4 || k == 6) lacc[k == 6] = mfma(ones, pf[j & 1][k == 6], j == 0 ? zero4() : lacc[k == 6]);
                              if (j == 15 && k == 9) linv = (f32x2){1.0f / lacc[0][0], 1.0f / lacc[1][0]};
                              if (k == 8 && (iw & 3) == 0) { of[(iw >> 2) & 1][0] = ldsr128(xb + 2048 * (iw >> 2)); of[(iw >> 2) & 1][1] = ldsr128(xb + 2048 * (iw >> 2) + 1024); }
                            },
                            [&]() {
                              pf[j & 1][0] = ldsr128(pb + (j & 1) * 6144); pf[j & 1][1] = ldsr128(pb + (j & 1) * 6144 + 1024);
                            });
          waitbar<8>();
        }
      }
#pragma unroll
      for (int m = 10 - D3; m < 10; ++m) { mm3(15, m, 0); mm3(15, m, 1); }
      rdT(8, 0, tbh, REG + 3 * SLOT, 6); rdT(8, 1, tbh, REG + 3 * SLOT, 6); rdT(9, 0, tbh, REG + 3 * SLOT, 7); rdT(9, 1, tbh, REG + 3 * SLOT, 7);
      bf16x8 og[8];
#pragma unroll
      for (int k = 0; k < 8; ++k) { const u32x4 w = packO(k); ldsw128(xb + 1024 * k, w); og[k] = __builtin_bit_cast(bf16x8, w); }
      mm3(0, 8, 0); mm3(0, 8, 1); mm3(0, 9, 0); mm3(0, 9, 1);
      bar();
      unsigned ln;
      asm volatile("v_mbcnt_lo_u32_b32 %0, -1, 0\n\tv_mbcnt_hi_u32_b32 %0, -1, %0" : "=v"(ln));
      const unsigned tl2 = tbh - 2 * REG;
#pragma unroll
      for (int j = 0; j < 4; ++j) {
        const int gi = (j + 1) & 3;
#pragma unroll
        for (int m = 0; m < 16; ++m) rdT(m >> 1, m & 1, m < 8 ? tl2 : tbh, ((m >> 2) & 1) * REG + j * SLOT, 6 + ((m >> 1) & 1));
#pragma unroll
        for (int m = 0; m < 8; ++m) {
          oa[gi][m & 1][0] = mfma(FA(m), og[m & 6], oa[gi][m & 1][0]);
          oa[gi][m & 1][1] = mfma(FA(m), og[(m & 6) + 1], oa[gi][m & 1][1]);
        }
        if (j < 3) {
          stP(ln, oa[gi][0][0], oa[gi][0][1], gi, 6); stP(ln, oa[gi][1][0], oa[gi][1][1], gi, 7);
          bar();
        }
      }
#pragma unroll
      for (int q = 0; q < 4; ++q) putF(ln, oa[0][q >> 1][q & 1], 12 + q);
      waitbar<0>();
      if (qh == 0) {
        if (ln < 3)
          __hip_atomic_store((GAS unsigned long long*)(ws + WS_FLAG + ((grp + ((hg + ln + 1) & 3)) * 4 + ln) * 128), tag, __ATOMIC_RELAXED, __HIP_MEMORY_SCOPE_AGENT);
        GAS unsigned long long* fl = (GAS unsigned long long*)(ws + WS_FLAG + (blockIdx.x * 4 + (ln < 3 ? ln : 0)) * 128);
        for (unsigned spins = 0; spins < (1u << 22); ++spins) {
          const unsigned long long v = __hip_atomic_load(fl, __ATOMIC_RELAXED, __HIP_MEMORY_SCOPE_AGENT);
          if (__all(v == tag)) break;
          __builtin_amdgcn_s_sleep(1);
        }
        if (hg == 0 && ln == 0) __hip_atomic_store(ep, tag, __ATOMIC_RELAXED, __HIP_MEMORY_SCOPE_AGENT);
      }
    } else {
      setup(true);
      { bf16x8 xf[16][2]; prologue(xf, 3, noextra); }
      dma_all(stO(2)); waitbar<8>();
      for (int s = 0; s < 17; ++s) waitbar<0>();
      dma_all(stO(20)); bar(); dma_all(stO(21)); bar(); dma_all(stO(22)); waitbar<8>();
      f32x4 oa[4][6][2];
      bf16x8 of[2][2];
#pragma unroll
      for (int i = 0; i < 48; ++i) oa[i / 12][(i % 12) >> 1][i & 1] = zero4();
      zfa();
      of[0][0] = of[0][1] = of[1][0] = of[1][1] = zero8();
      auto mm4 = [&](int nq, int par, int m, int t) { oa[nq][m][t] = mfma(FA(m), of[par][t], oa[nq][m][t]); };
      for (int u = 0; u < 7; ++u) {
#pragma unroll
        for (int i = 0; i < 16; ++i) {
          const int ip = (i + 15) & 15;
          const Dma db = stO(16 * u + 23 + i);
          pstep<6, D4, AH>([&](int m, int h) { rdT(m, h, tbh, REG + (i & 3) * SLOT, m); },
                           [&](int m, int t) { mm4(ip & 3, (ip >> 2) & 1, m, t); }, [&](int m, int t) { mm4(i & 3, (i >> 2) & 1, m, t); },
                           [&](int k, int h) { if (h == 0) dma_pos<6, 0>(db, k, (i & 1) == 1); },
                           [&]() { if ((i & 3) == 0) { of[(i >> 2) & 1][0] = ldsr128(xb + 2048 * (i >> 2)); of[(i >> 2) & 1][1] = ldsr128(xb + 2048 * (i >> 2) + 1024); } });
          waitbar<8>();
        }
      }
      STAMP(11);
      unsigned ln;
      asm volatile("v_mbcnt_lo_u32_b32 %0, -1, 0\n\tv_mbcnt_hi_u32_b32 %0, -1, %0" : "=v"(ln));
      const unsigned tl2 = tbh - 2 * REG;
#pragma unroll
      for (int j = 0; j < 4; ++j) {
        const int gi = (j + 1) & 3;
#pragma unroll
        for (int q = 0; q < 4; ++q) {
          pstep<6, D4, AH>([&](int m, int h) { rdT(m, h, q < 2 ? tl2 : tbh, (q & 1) * REG + j * SLOT, m); },
                           [&](int m, int t) { if (q > 0) mm4(gi, (q - 1) & 1, m, t); else mm4(j == 0 ? 3 : j, 1, m, t); },
                           [&](int m, int t) { mm4(gi, q & 1, m, t); },
                           [&](int k, int h) {
                             if (h == 1 && j > 0 && q == 0) stP(ln, oa[j][k][0], oa[j][k][1], j, k);
                           },
                           [&]() { of[q & 1][0] = ldsr128(xb + 2048 * q); of[q & 1][1] = ldsr128(xb + 2048 * q + 1024); });
          if (j == 3 && q == DRQ) waitbar<0>();
        }
        if (j < 3) bar();
      }
#pragma unroll
      for (int m = 6 - D4; m < 6; ++m) { mm4(0, 1, m, 0); mm4(0, 1, m, 1); }
#pragma unroll
      for (int n = 0; n < 12; ++n) putF(ln, oa[0][n >> 1][n & 1], n);
      STAMP(12);
    }
  }
  waitbar<0>();
  {
    unsigned ln;
    asm volatile("v_mbcnt_lo_u32_b32 %0, -1, 0\n\tv_mbcnt_hi_u32_b32 %0, -1, %0" : "=v"(ln));
    f32x4 a[4];
    u32x4 in[6];
#pragma unroll
    for (int j = 0; j < 3; ++j)
#pragma unroll
      for (int c = 0; c < 2; ++c)
        in[2 * j + c] = __builtin_amdgcn_raw_buffer_load_b128(sP, ln * 16 + c * 1024, (blockIdx.x * 3 + j) * 16384 + qh * 8192 + role * 2048, 16);
#pragma unroll
    for (int q = 0; q < 4; ++q) {
      a[q] = __builtin_bit_cast(f32x4, ldsr128(ldsb + role * REG + qh * SLOT + q * 1024 + ln * 16));
#pragma unroll
      for (int j = 0; j < 3; ++j) {
        const unsigned w0 = in[2 * j + (q >> 1)][2 * (q & 1)], w1 = in[2 * j + (q >> 1)][2 * (q & 1) + 1];
        a[q] += (f32x4){__uint_as_float(w0 << 16), __uint_as_float(w0 & 0xffff0000u), __uint_as_float(w1 << 16), __uint_as_float(w1 & 0xffff0000u)};
      }
    }
    char* orow = out + ((size_t)b * 64 + 32 * qh + (ln & 15)) * 2048 + p * 1024 + ((rot + hg) & 3) * 256 + role * 64 + ((ln & 16) * 2 + (ln >> 5) * 16);
    *(u32x4*)(orow) = swap16(pack8(a[0], a[2]));
    *(u32x4*)(orow + 16 * 2048) = swap16(pack8(a[1], a[3]));
  }
}

extern "C" void kernel_launch(void* const* d_in, const int* in_sizes, int n_in, void* d_out, int out_size, void* d_ws, size_t ws_size,
                              hipStream_t stream) {
  (void)in_sizes; (void)n_in; (void)out_size; (void)ws_size;
  (void)hipFuncSetAttribute((const void*)mha_fused, hipFuncAttributeMaxDynamicSharedMemorySize, LDS_BYTES);
  mha_fused<<<256, 512, LDS_BYTES, stream>>>((const char*)d_in[0], (const char*)d_in[1], (const char*)d_in[2], (const char*)d_in[3],
                                             (const char*)d_in[4], (const char*)d_in[5], (const char*)d_in[6], (const char*)d_in[7],
                                             (const char*)d_in[8], (const char*)d_in[9], (char*)d_ws, (char*)d_out);
}
```

```cpp
#include <hip/hip_runtime.h>
#include <hip/hip_bf16.h>

typedef __amdgpu_buffer_rsrc_t rsrc_t;
using bf16x8 = __attribute__((ext_vector_type(8))) short;
using s16x4 = __attribute__((ext_vector_type(4))) short;
using f32x4 = __attribute__((ext_vector_type(4))) float;
using f32x2 = __attribute__((ext_vector_type(2))) float;
using u32x4 = __attribute__((ext_vector_type(4))) unsigned;
using bf16x2_t = __attribute__((ext_vector_type(2))) __bf16;
typedef short v4i16_t __attribute__((ext_vector_type(4)));
#define LAS __attribute__((address_space(3)))
#define GAS __attribute__((address_space(1)))
#define DI __device__ __forceinline__
#define SBAR() __builtin_amdgcn_sched_barrier(0)
#define PIN(x) asm volatile("" : "+v"(x))

constexpr int SLOT = 8192, REG = 32768;
constexpr int XB = 4 * REG;
constexpr int PB = XB + 2 * 10240;
constexpr int LDS_BYTES = PB + 4 * 3072;
constexpr unsigned WS_FLAG = 32u << 20, WS_EPOCH = 33u << 20;
constexpr float SC = 0.08838834764831845f * 1.4426950408889634f;
constexpr unsigned OOR = 0x80000000u;
constexpr unsigned NOLDS = 0x30000u;
#define THR 24.0f
#define ABL_KV 1
#define ABL_W 1
#define KVNT true
#define PBF16 1
#define DRQ 1
#define WARM 1
#define WS0 2
#define ORD 4
#define ROLEMAP 0x3210
#define D1 3
#define D3 3
#define D4 2
#define AH 2

DI unsigned rfl(unsigned x) { return __builtin_amdgcn_readfirstlane(x); }
DI unsigned cvtpk(float lo, float hi) { f32x2 v = {lo, hi}; return __builtin_bit_cast(unsigned, __builtin_convertvector(v, bf16x2_t)); }
DI f32x4 mfma(bf16x8 a, bf16x8 b, f32x4 c) { return __builtin_amdgcn_mfma_f32_16x16x32_bf16(a, b, c, 0, 0, 0); }
DI s16x4 trd(unsigned addr) { return __builtin_bit_cast(s16x4, __builtin_amdgcn_ds_read_tr16_b64_v4i16((LAS v4i16_t*)(size_t)addr)); }
DI bf16x8 ldsr128(unsigned addr) { return *(const LAS bf16x8*)(size_t)addr; }
DI void ldsw128(unsigned addr, u32x4 v) { *(LAS u32x4*)(size_t)addr = v; }
DI bf16x8 cat(s16x4 a, s16x4 b) { return (bf16x8){a[0], a[1], a[2], a[3], b[0], b[1], b[2], b[3]}; }
DI f32x4 zero4() { return (f32x4){0.f, 0.f, 0.f, 0.f}; }
DI bf16x8 zero8() { return (bf16x8){0, 0, 0, 0, 0, 0, 0, 0}; }
DI u32x4 pack8(f32x4 a, f32x4 b) { return (u32x4){cvtpk(a[0], a[1]), cvtpk(a[2], a[3]), cvtpk(b[0], b[1]), cvtpk(b[2], b[3])}; }
DI u32x4 swap16(u32x4 w) {
  auto s0 = __builtin_amdgcn_permlane16_swap(w[0], w[2], false, false);
  auto s1 = __builtin_amdgcn_permlane16_swap(w[1], w[3], false, false);
  return (u32x4){s0[0], s1[0], s0[1], s1[1]};
}
DI float max3f(float a, float b, float c) { return __builtin_fmaxf(__builtin_fmaxf(a, b), c); }
DI float red_max(float v) {
  auto r = __builtin_amdgcn_permlane16_swap(__float_as_uint(v), __float_as_uint(v), false, false);
  v = __builtin_fmaxf(__uint_as_float(r[0]), __uint_as_float(r[1]));
  auto q = __builtin_amdgcn_permlane32_swap(__float_as_uint(v), __float_as_uint(v), false, false);
  return __builtin_fmaxf(__uint_as_float(q[0]), __uint_as_float(q[1]));
}
DI float red_sum(float v) {
  auto r = __builtin_amdgcn_permlane16_swap(__float_as_uint(v), __float_as_uint(v), false, false);
  v = __uint_as_float(r[0]) + __uint_as_float(r[1]);
  auto q = __builtin_amdgcn_permlane32_swap(__float_as_uint(v), __float_as_uint(v), false, false);
  return __uint_as_float(q[0]) + __uint_as_float(q[1]);
}
template <int N> DI void waitbar() { asm volatile("s_waitcnt vmcnt(%0) lgkmcnt(0)\n\ts_barrier" ::"i"(N) : "memory"); SBAR(); }
DI void bar() { asm volatile("s_waitcnt lgkmcnt(0)\n\ts_barrier" ::: "memory"); SBAR(); }

struct Dma { rsrc_t srd; unsigned m0, so, sstep, v0; bool nt; int kind; };
template <int J> DI void dma_piece(const Dma& d) {
  const unsigned v = d.v0 ^ (d.kind == 1 ? 64u * J : d.kind == 0 ? 128u * (J & 1) : 128u * (J >> 1));
  const unsigned so = d.so + J * d.sstep;
  if (J == 0) {
    if (d.nt) asm volatile("s_mov_b32 m0, %0\n\ts_nop 0\n\tbuffer_load_dwordx4 %1, %2, %3 offen offset:%4 nt lds" ::"s"(d.m0), "v"(v), "s"(d.srd), "s"(so), "i"(1024 * J) : "m0");
    else asm volatile("s_mov_b32 m0, %0\n\ts_nop 0\n\tbuffer_load_dwordx4 %1, %2, %3 offen offset:%4 lds" ::"s"(d.m0), "v"(v), "s"(d.srd), "s"(so), "i"(1024 * J) : "m0");
  } else {
    if (d.nt) asm volatile("buffer_load_dwordx4 %0, %1, %2 offen offset:%3 nt lds" ::"v"(v), "s"(d.srd), "s"(so), "i"(1024 * J));
    else asm volatile("buffer_load_dwordx4 %0, %1, %2 offen offset:%3 lds" ::"v"(v), "s"(d.srd), "s"(so), "i"(1024 * J));
  }
}
DI void dma_all(const Dma& d) { dma_piece<0>(d); dma_piece<1>(d); dma_piece<2>(d); dma_piece<3>(d); }
template <int K0, int KS> DI void dma_slot(const Dma& d, int k) {
  if (k == K0) dma_piece<0>(d);
  if (k == K0 + KS) dma_piece<1>(d);
  if (k == K0 + 2 * KS) dma_piece<2>(d);
  if (k == K0 + 3 * KS) dma_piece<3>(d);
}
template <int N, int PH> DI void dma_pos(const Dma& d, int k, bool early) {
  if (ORD == 0) { if (N >= 8) dma_slot<PH, 2>(d, k); else dma_slot<0, 1>(d, k); }
  else if (ORD == 1) { if (early) dma_slot<0, 1>(d, k); else dma_slot<N - 4, 1>(d, k); }
  else if (ORD == 2) { if (k == (early ? 0 : N - 1)) dma_all(d); }
  else if (ORD == 3) { const int k0 = early ? 0 : N - 2; if (k == k0) { dma_piece<0>(d); dma_piece<1>(d); } if (k == k0 + 1) { dma_piece<2>(d); dma_piece<3>(d); } }
  else if (ORD == 4) { if (k == 0) dma_all(d); }
  else if (ORD == 5) { if (k == (early ? 0 : N / 2)) dma_all(d); }
  else if (ORD == 6) { if (k == (early ? 0 : 2)) dma_all(d); }
}

struct NoPre { DI void operator()() const {} };
template <int N, int D, int A, class RD, class MP, class MC, class FL, class PR = NoPre>
DI void pstep(RD rd, MP mprev, MC mcur, FL fill, PR pre = PR()) {
#pragma unroll
  for (int k = 0; k < N; ++k) {
#pragma unroll
    for (int h = 0; h < 2; ++h) {
      if (k < D) mprev(N - D + k, h); else mcur(k - D, h);
      SBAR();
      if (k == 0) {
        if (h == 0) pre();
#pragma unroll
        for (int q = h; q < A; q += 2) { rd(q, 0); rd(q, 1); }
      }
      if (k + A < N) rd(k + A, h);
      fill(k, h);
      SBAR();
    }
  }
}

#define STAMP(i)

__global__ __launch_bounds__(512, 2) void mha_fused(const char* __restrict__ xA, const char* __restrict__ kA, const char* __restrict__ vA,
                                                    const char* __restrict__ wqA, const char* __restrict__ woA, const char* __restrict__ xB,
                                                    const char* __restrict__ kB, const char* __restrict__ vB, const char* __restrict__ wqB,
                                                    const char* __restrict__ woB, char* __restrict__ ws, char* __restrict__ out) {
  extern __shared__ __attribute__((aligned(16))) char lds[];
  unsigned lane = threadIdx.x & 63;
  const unsigned tid = threadIdx.x, wid = rfl(tid >> 6), role = (ROLEMAP >> (4 * (wid >> 1))) & 3, qh = wid & 1;
  const unsigned xg = blockIdx.x & 7, p = xg >> 2, hg = xg & 3, b = blockIdx.x >> 3;
  const unsigned rot = b >> 3;
  const char* kc = (p ? kB : kA) + (size_t)(b * 32 + hg * 8) * 131072;
  const char* vc = (p ? vB : vA) + (size_t)(b * 32 + hg * 8) * 131072;
  const char* wq = p ? wqB : wqA;
  const char* wo = p ? woB : woA;
  const char* xp = (p ? xB : xA) + (size_t)b * 65536;
  const rsrc_t sK = __builtin_amdgcn_make_buffer_rsrc((void*)kc, (short)0, ABL_KV << 20, 0x00020000);
  const rsrc_t sV = __builtin_amdgcn_make_buffer_rsrc((void*)vc, (short)0, ABL_KV << 20, 0x00020000);
  const rsrc_t sQ = __builtin_amdgcn_make_buffer_rsrc((void*)wq, (short)0, ABL_W << 22, 0x00020000);
  const rsrc_t sO = __builtin_amdgcn_make_buffer_rsrc((void*)wo, (short)0, ABL_W << 22, 0x00020000);
  const rsrc_t sX = __builtin_amdgcn_make_buffer_rsrc((void*)xp, (short)0, 65536, 0x00020000);
  const unsigned ldsb = (unsigned)(size_t)lds;
  unsigned g, r, dq, dO, dV, dK, tq, tbh, xb;
  auto setup = [&](bool opaque) {
    if (opaque) asm volatile("v_mbcnt_lo_u32_b32 %0, -1, 0\n\tv_mbcnt_hi_u32_b32 %0, -1, %0" : "=v"(lane));
    g = lane >> 4; r = lane & 15;
    const unsigned swT = 16 * (r ^ (2 * g));
    dq = g * 8192 + swT; dO = g * 1024 + swT; dV = g * 256 + swT; dK = g * 256 + 16 * (r ^ g);
    const unsigned q4 = r >> 2;
    tq = ldsb + (8 * g + q4) * 256 + 32 * (q4 | ((g & 1) << 2)) + 8 * (lane & 3);
    tbh = ldsb + 2 * REG + (lane >> 2) * 256 + 32 * ((lane >> 2) & 7) + 8 * (lane & 3);
    xb = ldsb + XB + qh * 10240 + lane * 16;
    if (opaque) { PIN(tbh); PIN(xb); }
  };
  setup(false);
  STAMP(0);
#pragma unroll
  for (int i = 0; i < 8; ++i) {
    const unsigned m0 = ldsb + role * REG + (1 + qh) * SLOT + i * 1024, so = (16 * role + 8 * qh + i) * 1024;
    const unsigned v = 16 * (((lane ^ (8 * qh + i)) + 16 * rot) & 63);
    asm volatile("s_mov_b32 m0, %0\n\ts_nop 0\n\tbuffer_load_dwordx4 %1, %2, %3 offen lds" ::"s"(m0), "v"(v), "s"(sX), "s"(so) : "m0");
  }
  const unsigned mq = ldsb + qh * 4096;
  auto head = [&](unsigned u) -> unsigned { return (u + b) & 7u; };
  auto mkWq = [&](unsigned u, unsigned i, unsigned reg, unsigned slot, bool nt = false) -> Dma {
    return Dma{sQ, mq + reg * REG + slot * SLOT, (32 * ((i + 4 * rot) & 15) + 16 * qh) * 8192 + (8 * hg + head(u)) * 256, 4 * 8192 - 1024, dq, nt, 2};
  };
  auto mkWo = [&](unsigned u, unsigned dblk, unsigned nq, unsigned reg, unsigned slot) -> Dma {
    return Dma{sO, mq + reg * REG + slot * SLOT, ((8 * hg + head(u)) * 128 + 32 * dblk + 16 * qh) * 1024 + ((nq + rot + hg) & 3) * 256, 4 * 1024 - 1024, dO, false, 0};
  };
  auto stK = [&](int sg) -> Dma {
    const bool ok = (unsigned)sg < 128u;
    return Dma{sK, ok ? mq + REG + (sg & 3) * SLOT : NOLDS, ok ? head(sg >> 4) * 131072 + (sg & 15) * 8192 + qh * 4096 : OOR, 0, dK, KVNT, 1};
  };
  auto stV = [&](int sg) -> Dma {
    const int t = sg - 3; const bool ok = (unsigned)t < 128u;
    return Dma{sV, ok ? mq + 2 * REG + (sg & 3) * SLOT : NOLDS, ok ? head(t >> 4) * 131072 + (t & 15) * 8192 + qh * 4096 : OOR, 0, dV, KVNT, 0};
  };
  auto stQ = [&](int sg) -> Dma {
    const int t = sg + 17; const bool ok = t < 128;
    Dma d = mkWq(t >> 4, t & 15, 0, sg & 3);
    d.m0 = ok ? d.m0 : NOLDS; d.so = ok ? d.so : OOR;
    return d;
  };
  auto stO = [&](int sg) -> Dma {
    const int t = sg - 20;
    Dma d = mkWo(t >> 4, (t >> 2) & 3, t & 3, 3, sg & 3);
    d.m0 = t < 112 ? d.m0 : NOLDS; d.so = (unsigned)t < 112u ? d.so : OOR;
    return d;
  };
  { Dma w = mkWo(0, role, 0, role, 0); if (WARM < 4) { w.so = OOR; w.m0 = NOLDS; } w.nt = WARM == 5; dma_all(w); }
  asm volatile("s_barrier" ::: "memory");
  dma_all(mkWq(0, role, role, 3, true));
  s16x4 fh[10][2];
  auto FA = [&](int f) { return cat(fh[f][0], fh[f][1]); };
  auto rdT = [&](int f, int h, unsigned tb, unsigned off, int ct) { fh[f][h] = trd((tb ^ (32u * ct)) + (off + 4096 * h)); };
  auto rdQ = [&](int f, int h, unsigned tb, unsigned off, int ct) { fh[f][h] = trd((tb ^ (32u * ct)) + (off + 1024 * h)); };
  auto nop = [&](int, int) {};
  auto zfa = [&]() {
#pragma unroll
    for (int i = 0; i < 10; ++i) fh[i][0] = fh[i][1] = (s16x4){0, 0, 0, 0};
  };
  auto prologue = [&](bf16x8 (*xf)[2], const int R, auto extra) {
    waitbar<8>();
    {
      const Dma f4 = mkWq(0, 4 + R, R, 0);
      const unsigned xa = ldsb + 2 * qh * REG + (1 + (r >> 3)) * SLOT + (r & 7) * 1024 + 16 * (g ^ r);
#pragma unroll
      for (int i = 0; i < 16; ++i) {
        xf[i][0] = ldsr128((xa ^ (64u * (i & 3))) + 256 * (i >> 2));
        xf[i][1] = ldsr128((xa ^ (64u * (i & 3))) + 256 * (i >> 2) + REG);
        dma_slot<3, 4>(f4, i);
      }
    }
    waitbar<4>();
    if (R == 0) STAMP(1);
    f32x4 q0[2][2] = {{zero4(), zero4()}, {zero4(), zero4()}};
    unsigned tqh = tq + 2 * REG;
    PIN(tqh);
    auto mm0 = [&](int j, int m, int t) { q0[m & 1][t] = mfma(FA(m), xf[4 * j + (m >> 1)][t], q0[m & 1][t]); };
#pragma unroll
    for (int j = 0; j < 4; ++j) {
      if (j == 0) { dma_all(mkWq(0, 8 + R, R, 1)); dma_all(mkWq(0, 12 + R, R, 2)); }
      if (j >= 1) { const int sg = j - 2; dma_all(R == 0 ? stQ(sg) : R == 1 ? stK(sg) : R == 2 ? stV(sg) : stO(sg)); }
      pstep<8, 3, 2>([&](int m, int h) { rdQ(m, h, m < 4 ? tq : tqh, ((m >> 1) & 1) * REG + ((j + 3) & 3) * SLOT, 2 * R + (m & 1)); },
                     [&](int m, int t) { if (j > 0) mm0(j - 1, m, t); }, [&](int m, int t) { mm0(j, m, t); }, nop);
      if (j == 0) extra();
      if (R == 2) { if (j < 3) waitbar<25>(); else bar(); } else waitbar<8>();
    }
#pragma unroll
    for (int m = 10; m < 16; ++m) mm0(3, m >> 1, m & 1);
    ldsw128(xb + 2048 * R + (R == 3 ? 2048 : 0), swap16(pack8(q0[0][0] * SC, q0[1][0] * SC)));
    ldsw128(xb + 2048 * R + (R == 3 ? 2048 : 0) + 1024, swap16(pack8(q0[0][1] * SC, q0[1][1] * SC)));
    SBAR();
  };
  auto noextra = [&]() {};
  const rsrc_t sP = __builtin_amdgcn_make_buffer_rsrc((void*)ws, (short)0, WS_FLAG, 0x00020000);

  if (role == 0) {
    setup(true);
    bf16x8 xf[16][2];
    prologue(xf, 0, noextra);
    f32x4 qacc[8][2];
#pragma unroll
    for (int i = 0; i < 8; ++i) qacc[i][0] = qacc[i][1] = zero4();
    auto mmQ = [&](int i, int m, int t) { qacc[m][t] = mfma(FA(m), xf[i][t], i == 0 ? zero4() : qacc[m][t]); };
    auto putQ = [&](int k) {
      ldsw128(xb + 1024 * (k < 6 ? k : k + 2), swap16(pack8(qacc[k & 6][k & 1] * SC, qacc[(k & 6) + 1][k & 1] * SC)));
    };
    for (int u = 1; u < 8; ++u) {
#pragma unroll
      for (int i = 0; i < 16; ++i) {
        const Dma da = stQ(16 * (u - 1) + i + 2);
        pstep<8, D1, AH>([&](int m, int h) { rdQ(m, h, tq, ((i + 3) & 3) * SLOT, m); },
                         [&](int m, int t) { mmQ((i + 15) & 15, m, t); }, [&](int m, int t) { mmQ(i, m, t); },
                         [&](int k, int h) { if (h == 0) dma_pos<8, 0>(da, k, (i & 1) == 0); if (h == 1 && i == 0 && u > 1) putQ(k); });
        waitbar<8>();
      }
    }
#pragma unroll
    for (int m = 8 - D1; m < 8; ++m) { mmQ(15, m, 0); mmQ(15, m, 1); }
#pragma unroll
    for (int k = 0; k < 8; ++k) putQ(k);
    bar();
#pragma unroll
    for (int j = 0; j < 4; ++j) { dma_all(mkWo(7, 0, j + 1, 0, j)); bar(); bar(); bar(); bar(); }
    dma_all(mkWo(7, 1, 1, 1, 0)); dma_all(mkWo(7, 1, 2, 1, 1)); bar();
    dma_all(mkWo(7, 2, 1, 2, 0)); dma_all(mkWo(7, 3, 1, 3, 0)); dma_all(mkWo(7, 1, 3, 1, 2)); bar();
    dma_all(mkWo(7, 2, 2, 2, 1)); dma_all(mkWo(7, 3, 2, 3, 1)); dma_all(mkWo(7, 1, 0, 1, 3)); bar();
    dma_all(mkWo(7, 2, 3, 2, 2)); dma_all(mkWo(7, 3, 3, 3, 2)); dma_all(mkWo(7, 2, 0, 2, 3)); waitbar<28>();
    dma_all(mkWo(7, 3, 0, 3, 3)); waitbar<20>();
    waitbar<8>();
    waitbar<0>();
    bar();
  } else if (role == 1) {
    setup(true);
    { bf16x8 xf[16][2]; prologue(xf, 1, noextra); }
    dma_all(stK(2)); waitbar<8>();
    const unsigned sig = ((g & 1) << 1) | (g >> 1);
    const unsigned kb = ldsb + REG + r * 256 + 16 * (sig ^ r);
    unsigned pb = ldsb + PB + qh * 3072 + lane * 16;
    PIN(pb);
    f32x4 sb[4][2][2], negm[2] = {zero4(), zero4()};
    bf16x8 qf[4][2], fa[8];
#pragma unroll
    for (int i = 0; i < 16; ++i) sb[i >> 2][(i >> 1) & 1][i & 1] = zero4();
#pragma unroll
    for (int i = 0; i < 8; ++i) { qf[i >> 1][i & 1] = zero8(); fa[i] = zero8(); }
    float mx[2] = {0.f, 0.f}, mc = 0.f;
    unsigned flag = 0;
    auto mmK = [&](int i, int m, int t) {
      sb[i & 3][m >> 2][t] = mfma(fa[m], qf[m & 3][t], (m & 3) == 0 ? (i < 2 ? zero4() : negm[t]) : sb[i & 3][m >> 2][t]);
    };
    auto soft = [&](int i, int k, int h, bool chk) {
      const int je = (i + 14) & 15, jm = (i + 15) & 15;
      auto& se = sb[je & 3];
      auto& sm = sb[jm & 3];
      auto g0 = [](int q) { return q < 4 ? 3 * q : 12 + 2 * (q - 4); };
      auto gn = [](int q) { return q < 4 ? 3 : 2; };
      if (h == 0 && k < 6) {
#pragma unroll
        for (int e = g0(k); e < g0(k) + gn(k); ++e) se[(e >> 2) & 1][e >> 3][e & 3] = __builtin_amdgcn_exp2f(se[(e >> 2) & 1][e >> 3][e & 3]);
      }
      if (h == 0) return;
      if (k == 3) { u32x4 w = pack8(se[0][0], se[1][0]); w[0] |= flag; ldsw128(pb + (je & 1) * 6144, w); }
      if (k == 6) ldsw128(pb + (je & 1) * 6144 + 1024, pack8(se[0][1], se[1][1]));
      if (chk) {
        if (k == 3) { mx[0] = max3f(sm[0][0][0], sm[0][0][1], sm[0][0][2]); mx[1] = max3f(sm[0][1][0], sm[0][1][1], sm[0][1][2]); PIN(mx[0]); PIN(mx[1]); }
        if (k == 4) { mx[0] = max3f(mx[0], sm[0][0][3], sm[1][0][0]); mx[1] = max3f(mx[1], sm[0][1][3], sm[1][1][0]); PIN(mx[0]); PIN(mx[1]); }
        if (k == 5) { mx[0] = max3f(mx[0], sm[1][0][1], sm[1][0][2]); mx[1] = max3f(mx[1], sm[1][1][1], sm[1][1][2]); PIN(mx[0]); PIN(mx[1]); }
        if (k == 6 && jm != 0) { mc = max3f(sm[1][0][3], sm[1][1][3], mx[0]); PIN(mc); mc = max3f(mc, mx[1], 0.f); }
        if (k == 7) {
          flag = 0;
          if (jm == 0) {
#pragma unroll
            for (int t = 0; t < 2; ++t) {
              const float d = red_max(__builtin_fmaxf(mx[t], sm[1][t][3]));
              sm[0][t] -= d; sm[1][t] -= d; sb[i & 3][0][t] -= d; sb[i & 3][1][t] -= d;
              negm[t] = (f32x4){-d, -d, -d, -d};
            }
          } else if (__builtin_expect(__builtin_amdgcn_ballot_w64(mc > THR) != 0, 0)) {
            flag = 0x80000000u;
            f32x2 al;
#pragma unroll
            for (int t = 0; t < 2; ++t) {
              const float d = __builtin_fmaxf(red_max(__builtin_fmaxf(mx[t], sm[1][t][3])), 0.f);
              al[t] = __builtin_amdgcn_exp2f(-d);
              sm[0][t] -= d; sm[1][t] -= d;
              if (jm != 15) { sb[i & 3][0][t] -= d; sb[i & 3][1][t] -= d; negm[t] -= d; }
            }
            *(LAS f32x2*)(size_t)(pb + (jm & 1) * 6144 + 2048 + 8 * ((jm >> 1) & 1)) = al;
          }
        }
      }
    };
    for (int u = 0; u < 8; ++u) {
      STAMP(2 + u);
#pragma unroll
      for (int i = 0; i < 16; ++i) {
        const Dma db = stK(16 * u + i + 3);
        pstep<8, 3, AH>([&](int m, int h) { if (h == 0) fa[m] = ldsr128((kb ^ (64u * (m & 3))) + ((i & 3) * SLOT + 4096 * (m >> 2))); },
                        [&](int m, int t) { mmK((i + 15) & 15, m, t); }, [&](int m, int t) { mmK(i, m, t); },
                        [&](int k, int h) {
                          if (h == 1 && i == 0 && k < 3) {
                            qf[k + 1][0] = ldsr128(xb + 2048 * (k < 2 ? k + 1 : 4)); qf[k + 1][1] = ldsr128(xb + 2048 * (k < 2 ? k + 1 : 4) + 1024);
                          }
                          if (h == 1) dma_pos<8, 1>(db, k, (i & 1) == 0);
                          soft(i, k, h, true);
                        },
                        [&]() { if (i == 0) { qf[0][0] = ldsr128(xb); qf[0][1] = ldsr128(xb + 1024); } });
        waitbar<8>();
      }
    }
    STAMP(10);
#pragma unroll
    for (int m = 10; m < 16; ++m) mmK(15, m >> 1, m & 1);
#pragma unroll
    for (int k = 0; k < 16; ++k) soft(0, k >> 1, k & 1, true);
    bar();
#pragma unroll
    for (int k = 0; k < 16; ++k) soft(1, k >> 1, k & 1, false);
    bar();
    bar(); bar(); bar(); bar(); bar();
    bar();
  } else {
    const unsigned grp = blockIdx.x & ~3u;
    auto stP = [&](unsigned ln, f32x4 a0, f32x4 a1, int gi, int ct) {
      const unsigned so = ((grp + ((hg + gi) & 3)) * 3 + gi - 1) * 16384 + qh * 8192 + (ct >> 2) * 4096;
      __builtin_amdgcn_raw_buffer_store_b128(pack8(a0, a1), sP, ln * 16 + (ct & 3) * 1024, so, 16);
    };
    auto putF = [&](unsigned ln, f32x4 a, int n) { ldsw128(ldsb + (n >> 2) * REG + qh * SLOT + (n & 3) * 1024 + ln * 16, __builtin_bit_cast(u32x4, a)); };
    if (role == 2) {
      setup(true);
#pragma unroll
      for (int i = 0; i < 10; ++i) ldsw128(xb + 1024 * i, (u32x4){0, 0, 0, 0});
      GAS unsigned long long* ep = (GAS unsigned long long*)(ws + WS_EPOCH + (blockIdx.x >> 2) * 128);
      unsigned long long tag;
      u32x4 wm[16];
      {
        bf16x8 xf[16][2];
        prologue(xf, 2, [&]() {
          asm volatile("global_load_dwordx2 %0, %1, off sc1" : "=&v"(tag) : "v"(ep) : "memory");
#pragma unroll
          for (int i = 0; i < 16; ++i) {
            const unsigned so = (WARM == 1 || WARM == 5) ? ((8 * hg + head(0)) * 128 + 64 * qh + 4 * i) * 1024 + ((rot + hg) & 3) * 256 : OOR;
            asm volatile("buffer_load_dwordx4 %0, %1, %2, %3 offen" : "=&v"(wm[i]) : "v"(dO), "s"(sO), "s"(so) : "memory");
          }
        });
      }
      bar();
      dma_all(stV(3)); bar();
      dma_all(stV(4)); bar();
      dma_all(stV(5));
      asm volatile("s_waitcnt vmcnt(8) lgkmcnt(0)\n\ts_barrier"
                   : "+v"(tag), "+v"(wm[0]), "+v"(wm[1]), "+v"(wm[2]), "+v"(wm[3]), "+v"(wm[4]), "+v"(wm[5]), "+v"(wm[6]), "+v"(wm[7]), "+v"(wm[8]), "+v"(wm[9]),
                     "+v"(wm[10]), "+v"(wm[11]), "+v"(wm[12]), "+v"(wm[13]), "+v"(wm[14]), "+v"(wm[15])::"memory");
      tag = ((unsigned long long)rfl((unsigned)(tag >> 32)) << 32) | rfl((unsigned)tag);
      tag = (tag ^ (tag >> 31)) * 0x9E3779B97F4A7C15ull + 0x632BE59BD9B4E019ull;
      unsigned pb = ldsb + PB + qh * 3072 + lane * 16;
      PIN(pb);
      f32x4 o[8][2], oa[4][2][2];
      bf16x8 pf[2][2], of[2][2];
#pragma unroll
      for (int i = 0; i < 16; ++i) { o[i >> 1][i & 1] = zero4(); oa[i >> 2][(i >> 1) & 1][i & 1] = zero4(); }
      zfa();
      pf[0][0] = pf[0][1] = pf[1][0] = pf[1][1] = of[0][0] = of[0][1] = of[1][0] = of[1][1] = zero8();
      f32x2 linv = {0.f, 0.f};
      f32x4 lacc[2] = {zero4(), zero4()};
      bf16x8 ones = {0x3f80, 0x3f80, 0x3f80, 0x3f80, 0x3f80, 0x3f80, 0x3f80, 0x3f80};
      PIN(ones);
      auto mm3 = [&](int j, int m, int t) {
        if (m < 8) o[m][t] = mfma(FA(m), pf[j & 1][t], j == 0 ? zero4() : o[m][t]);
        else { const int iw = (j + 15) & 15; oa[iw & 3][m & 1][t] = mfma(FA(m), of[(iw >> 2) & 1][t], oa[iw & 3][m & 1][t]); }
      };
      auto packO = [&](int k) { return pack8(o[k & 6][k & 1] * linv[k & 1], o[(k & 6) + 1][k & 1] * linv[k & 1]); };
      for (int u = 0; u < 8; ++u) {
#pragma unroll
        for (int j = 0; j < 16; ++j) {
          const int iw = (j + 15) & 15;
          const Dma da = stV(16 * u + 6 + j);
          pstep<10, D3, AH>([&](int m, int h) { if (m < 8) rdT(m, h, tbh, ((j + 3) & 3) * SLOT, m); else rdT(m, h, tbh, REG + ((j + 3) & 3) * SLOT, m - 2); },
                            [&](int m, int t) { mm3((j + 15) & 15, m, t); }, [&](int m, int t) { mm3(j, m, t); },
                            [&](int k, int h) {
                              if (h == 0) { dma_pos<10, 1>(da, k, (j & 1) == 1); return; }
                              if (j == 0 && k < 8) ldsw128(xb + 1024 * k, packO(k));
                              if (j > 0 && k == D3 - 1) {
                                if (__builtin_expect(__builtin_amdgcn_ballot_w64((int)__builtin_bit_cast(u32x4, pf[j & 1][0])[0] < 0) != 0, 0)) {
                                  const f32x2 alpha = *(const LAS f32x2*)(size_t)(pb + (j & 1) * 6144 + 2048 + 8 * ((j >> 1) & 1));
                                  { u32x4 w = __builtin_bit_cast(u32x4, pf[j & 1][0]); w[0] &= 0x7fffffffu; pf[j & 1][0] = __builtin_bit_cast(bf16x8, w); }
                                  f32x4 a0 = {alpha[0], alpha[0], alpha[0], alpha[0]}, a1 = {alpha[1], alpha[1], alpha[1], alpha[1]};
                                  PIN(a0); PIN(a1);
#pragma unroll
                                  for (int ct = 0; ct < 8; ++ct) { o[ct][0] *= a0; o[ct][1] *= a1; }
                                  lacc[0] *= a0; lacc[1] *= a1;
                                }
                              }
                              if (k == 4 || k == 6) lacc[k == 6] = mfma(ones, pf[j & 1][k == 6], j == 0 ? zero4() : lacc[k == 6]);
                              if (j == 15 && k == 9) linv = (f32x2){1.0f / lacc[0][0], 1.0f / lacc[1][0]};
                              if (k == 8 && (iw & 3) == 0) { of[(iw >> 2) & 1][0] = ldsr128(xb + 2048 * (iw >> 2)); of[(iw >> 2) & 1][1] = ldsr128(xb + 2048 * (iw >> 2) + 1024); }
                            },
                            [&]() {
                              pf[j & 1][0] = ldsr128(pb + (j & 1) * 6144); pf[j & 1][1] = ldsr128(pb + (j & 1) * 6144 + 1024);
                            });
          waitbar<8>();
        }
      }
#pragma unroll
      for (int m = 10 - D3; m < 10; ++m) { mm3(15, m, 0); mm3(15, m, 1); }
      rdT(8, 0, tbh, REG + 3 * SLOT, 6); rdT(8, 1, tbh, REG + 3 * SLOT, 6); rdT(9, 0, tbh, REG + 3 * SLOT, 7); rdT(9, 1, tbh, REG + 3 * SLOT, 7);
      bf16x8 og[8];
#pragma unroll
      for (int k = 0; k < 8; ++k) { const u32x4 w = packO(k); ldsw128(xb + 1024 * k, w); og[k] = __builtin_bit_cast(bf16x8, w); }
      mm3(0, 8, 0); mm3(0, 8, 1); mm3(0, 9, 0); mm3(0, 9, 1);
      bar();
      unsigned ln;
      asm volatile("v_mbcnt_lo_u32_b32 %0, -1, 0\n\tv_mbcnt_hi_u32_b32 %0, -1, %0" : "=v"(ln));
      const unsigned tl2 = tbh - 2 * REG;
#pragma unroll
      for (int j = 0; j < 4; ++j) {
        const int gi = (j + 1) & 3;
#pragma unroll
        for (int m = 0; m < 16; ++m) rdT(m >> 1, m & 1, m < 8 ? tl2 : tbh, ((m >> 2) & 1) * REG + j * SLOT, 6 + ((m >> 1) & 1));
#pragma unroll
        for (int m = 0; m < 8; ++m) {
          oa[gi][m & 1][0] = mfma(FA(m), og[m & 6], oa[gi][m & 1][0]);
          oa[gi][m & 1][1] = mfma(FA(m), og[(m & 6) + 1], oa[gi][m & 1][1]);
        }
        if (j < 3) {
          stP(ln, oa[gi][0][0], oa[gi][0][1], gi, 6); stP(ln, oa[gi][1][0], oa[gi][1][1], gi, 7);
          bar();
        }
      }
#pragma unroll
      for (int q = 0; q < 4; ++q) putF(ln, oa[0][q >> 1][q & 1], 12 + q);
      waitbar<0>();
      if (qh == 0) {
        if (ln < 3)
          __hip_atomic_store((GAS unsigned long long*)(ws + WS_FLAG + ((grp + ((hg + ln + 1) & 3)) * 4 + ln) * 128), tag, __ATOMIC_RELAXED, __HIP_MEMORY_SCOPE_AGENT);
        GAS unsigned long long* fl = (GAS unsigned long long*)(ws + WS_FLAG + (blockIdx.x * 4 + (ln < 3 ? ln : 0)) * 128);
        for (unsigned spins = 0; spins < (1u << 22); ++spins) {
          const unsigned long long v = __hip_atomic_load(fl, __ATOMIC_RELAXED, __HIP_MEMORY_SCOPE_AGENT);
          if (__all(v == tag)) break;
          __builtin_amdgcn_s_sleep(1);
        }
        if (hg == 0 && ln == 0) __hip_atomic_store(ep, tag, __ATOMIC_RELAXED, __HIP_MEMORY_SCOPE_AGENT);
      }
    } else {
      setup(true);
      { bf16x8 xf[16][2]; prologue(xf, 3, noextra); }
      dma_all(stO(2)); waitbar<8>();
      for (int s = 0; s < 17; ++s) waitbar<0>();
      dma_all(stO(20)); bar(); dma_all(stO(21)); bar(); dma_all(stO(22)); waitbar<8>();
      f32x4 oa[4][6][2];
      bf16x8 of[2][2];
#pragma unroll
      for (int i = 0; i < 48; ++i) oa[i / 12][(i % 12) >> 1][i & 1] = zero4();
      zfa();
      of[0][0] = of[0][1] = of[1][0] = of[1][1] = zero8();
      auto mm4 = [&](int nq, int par, int m, int t) { oa[nq][m][t] = mfma(FA(m), of[par][t], oa[nq][m][t]); };
      for (int u = 0; u < 7; ++u) {
#pragma unroll
        for (int i = 0; i < 16; ++i) {
          const int ip = (i + 15) & 15;
          const Dma db = stO(16 * u + 23 + i);
          pstep<6, D4, AH>([&](int m, int h) { rdT(m, h, tbh, REG + (i & 3) * SLOT, m); },
                           [&](int m, int t) { mm4(ip & 3, (ip >> 2) & 1, m, t); }, [&](int m, int t) { mm4(i & 3, (i >> 2) & 1, m, t); },
                           [&](int k, int h) { if (h == 0) dma_pos<6, 0>(db, k, (i & 1) == 1); },
                           [&]() { if ((i & 3) == 0) { of[(i >> 2) & 1][0] = ldsr128(xb + 2048 * (i >> 2)); of[(i >> 2) & 1][1] = ldsr128(xb + 2048 * (i >> 2) + 1024); } });
          waitbar<8>();
        }
      }
      STAMP(11);
      unsigned ln;
      asm volatile("v_mbcnt_lo_u32_b32 %0, -1, 0\n\tv_mbcnt_hi_u32_b32 %0, -1, %0" : "=v"(ln));
      const unsigned tl2 = tbh - 2 * REG;
#pragma unroll
      for (int j = 0; j < 4; ++j) {
        const int gi = (j + 1) & 3;
#pragma unroll
        for (int q = 0; q < 4; ++q) {
          pstep<6, D4, AH>([&](int m, int h) { rdT(m, h, q < 2 ? tl2 : tbh, (q & 1) * REG + j * SLOT, m); },
                           [&](int m, int t) { if (q > 0) mm4(gi, (q - 1) & 1, m, t); else mm4(j == 0 ? 3 : j, 1, m, t); },
                           [&](int m, int t) { mm4(gi, q & 1, m, t); },
                           [&](int k, int h) {
                             if (h == 1 && j > 0 && q == 0) stP(ln, oa[j][k][0], oa[j][k][1], j, k);
                           },
                           [&]() { of[q & 1][0] = ldsr128(xb + 2048 * q); of[q & 1][1] = ldsr128(xb + 2048 * q + 1024); });
          if (j == 3 && q == DRQ) waitbar<0>();
        }
        if (j < 3) bar();
      }
#pragma unroll
      for (int m = 6 - D4; m < 6; ++m) { mm4(0, 1, m, 0); mm4(0, 1, m, 1); }
#pragma unroll
      for (int n = 0; n < 12; ++n) putF(ln, oa[0][n >> 1][n & 1], n);
      STAMP(12);
    }
  }
  waitbar<0>();
  {
    unsigned ln;
    asm volatile("v_mbcnt_lo_u32_b32 %0, -1, 0\n\tv_mbcnt_hi_u32_b32 %0, -1, %0" : "=v"(ln));
    f32x4 a[4];
    u32x4 in[6];
#pragma unroll
    for (int j = 0; j < 3; ++j)
#pragma unroll
      for (int c = 0; c < 2; ++c)
        in[2 * j + c] = __builtin_amdgcn_raw_buffer_load_b128(sP, ln * 16 + c * 1024, (blockIdx.x * 3 + j) * 16384 + qh * 8192 + role * 2048, 16);
#pragma unroll
    for (int q = 0; q < 4; ++q) {
      a[q] = __builtin_bit_cast(f32x4, ldsr128(ldsb + role * REG + qh * SLOT + q * 1024 + ln * 16));
#pragma unroll
      for (int j = 0; j < 3; ++j) {
        const unsigned w0 = in[2 * j + (q >> 1)][2 * (q & 1)], w1 = in[2 * j + (q >> 1)][2 * (q & 1) + 1];
        a[q] += (f32x4){__uint_as_float(w0 << 16), __uint_as_float(w0 & 0xffff0000u), __uint_as_float(w1 << 16), __uint_as_float(w1 & 0xffff0000u)};
      }
    }
    char* orow = out + ((size_t)b * 64 + 32 * qh + (ln & 15)) * 2048 + p * 1024 + ((rot + hg) & 3) * 256 + role * 64 + ((ln & 16) * 2 + (ln >> 5) * 16);
    *(u32x4*)(orow) = swap16(pack8(a[0], a[2]));
    *(u32x4*)(orow + 16 * 2048) = swap16(pack8(a[1], a[3]));
  }
}

extern "C" void kernel_launch(void* const* d_in, const int* in_sizes, int n_in, void* d_out, int out_size, void* d_ws, size_t ws_size,
                              hipStream_t stream) {
  (void)in_sizes; (void)n_in; (void)out_size; (void)ws_size;
  (void)hipFuncSetAttribute((const void*)mha_fused, hipFuncAttributeMaxDynamicSharedMemorySize, LDS_BYTES);
  mha_fused<<<256, 512, LDS_BYTES, stream>>>((const char*)d_in[0], (const char*)d_in[1], (const char*)d_in[2], (const char*)d_in[3],
                                             (const char*)d_in[4], (const char*)d_in[5], (const char*)d_in[6], (const char*)d_in[7],
                                             (const char*)d_in[8], (const char*)d_in[9], (char*)d_ws, (char*)d_out);
}
```

```cpp
#include <hip/hip_runtime.h>
#include <hip/hip_bf16.h>

typedef __amdgpu_buffer_rsrc_t rsrc_t;
using bf16x8 = __attribute__((ext_vector_type(8))) short;
using s16x4 = __attribute__((ext_vector_type(4))) short;
using f32x4 = __attribute__((ext_vector_type(4))) float;
using f32x2 = __attribute__((ext_vector_type(2))) float;
using u32x4 = __attribute__((ext_vector_type(4))) unsigned;
using bf16x2_t = __attribute__((ext_vector_type(2))) __bf16;
typedef short v4i16_t __attribute__((ext_vector_type(4)));
#define LAS __attribute__((address_space(3)))
#define GAS __attribute__((address_space(1)))
#define DI __device__ __forceinline__
#define SBAR() __builtin_amdgcn_sched_barrier(0)
#define PIN(x) asm volatile("" : "+v"(x))

constexpr int SLOT = 8192, REG = 32768;
constexpr int XB = 4 * REG;
constexpr int PB = XB + 2 * 10240;
constexpr int LDS_BYTES = PB + 4 * 3072;
constexpr unsigned WS_FLAG = 32u << 20, WS_EPOCH = 33u << 20;
constexpr float SC = 0.08838834764831845f * 1.4426950408889634f;
constexpr unsigned OOR = 0x80000000u;
constexpr unsigned NOLDS = 0x30000u;
#define THR 24.0f
constexpr int D1 = 3, D3 = 3, D4 = 2, AH = 2;

DI unsigned rfl(unsigned x) { return __builtin_amdgcn_readfirstlane(x); }
DI unsigned cvtpk(float lo, float hi) { f32x2 v = {lo, hi}; return __builtin_bit_cast(unsigned, __builtin_convertvector(v, bf16x2_t)); }
DI f32x4 mfma(bf16x8 a, bf16x8 b, f32x4 c) { return __builtin_amdgcn_mfma_f32_16x16x32_bf16(a, b, c, 0, 0, 0); }
DI s16x4 trd(unsigned addr) { return __builtin_bit_cast(s16x4, __builtin_amdgcn_ds_read_tr16_b64_v4i16((LAS v4i16_t*)(size_t)addr)); }
DI bf16x8 ldsr128(unsigned addr) { return *(const LAS bf16x8*)(size_t)addr; }
DI void ldsw128(unsigned addr, u32x4 v) { *(LAS u32x4*)(size_t)addr = v; }
DI bf16x8 cat(s16x4 a, s16x4 b) { return (bf16x8){a[0], a[1], a[2], a[3], b[0], b[1], b[2], b[3]}; }
DI f32x4 zero4() { return (f32x4){0.f, 0.f, 0.f, 0.f}; }
DI bf16x8 zero8() { return (bf16x8){0, 0, 0, 0, 0, 0, 0, 0}; }
DI u32x4 pack8(f32x4 a, f32x4 b) { return (u32x4){cvtpk(a[0], a[1]), cvtpk(a[2], a[3]), cvtpk(b[0], b[1]), cvtpk(b[2], b[3])}; }
DI u32x4 swap16(u32x4 w) {
  auto s0 = __builtin_amdgcn_permlane16_swap(w[0], w[2], false, false);
  auto s1 = __builtin_amdgcn_permlane16_swap(w[1], w[3], false, false);
  return (u32x4){s0[0], s1[0], s0[1], s1[1]};
}
DI float max3f(float a, float b, float c) { return __builtin_fmaxf(__builtin_fmaxf(a, b), c); }
DI float red_max(float v) {
  auto r = __builtin_amdgcn_permlane16_swap(__float_as_uint(v), __float_as_uint(v), false, false);
  v = __builtin_fmaxf(__uint_as_float(r[0]), __uint_as_float(r[1]));
  auto q = __builtin_amdgcn_permlane32_swap(__float_as_uint(v), __float_as_uint(v), false, false);
  return __builtin_fmaxf(__uint_as_float(q[0]), __uint_as_float(q[1]));
}
template <int N> DI void waitbar() { asm volatile("s_waitcnt vmcnt(%0) lgkmcnt(0)\n\ts_barrier" ::"i"(N) : "memory"); SBAR(); }
DI void bar() { asm volatile("s_waitcnt lgkmcnt(0)\n\ts_barrier" ::: "memory"); SBAR(); }

struct Dma { rsrc_t srd; unsigned m0, so, sstep, v0; bool nt; int kind; };
template <int J> DI void dma_piece(const Dma& d) {
  const unsigned v = d.v0 ^ (d.kind == 1 ? 64u * J : d.kind == 0 ? 128u * (J & 1) : 128u * (J >> 1));
  const unsigned so = d.so + J * d.sstep;
  if (J == 0) {
    if (d.nt) asm volatile("s_mov_b32 m0, %0\n\ts_nop 0\n\tbuffer_load_dwordx4 %1, %2, %3 offen offset:%4 nt lds" ::"s"(d.m0), "v"(v), "s"(d.srd), "s"(so), "i"(1024 * J) : "m0");
    else asm volatile("s_mov_b32 m0, %0\n\ts_nop 0\n\tbuffer_load_dwordx4 %1, %2, %3 offen offset:%4 lds" ::"s"(d.m0), "v"(v), "s"(d.srd), "s"(so), "i"(1024 * J) : "m0");
  } else {
    if (d.nt) asm volatile("buffer_load_dwordx4 %0, %1, %2 offen offset:%3 nt lds" ::"v"(v), "s"(d.srd), "s"(so), "i"(1024 * J));
    else asm volatile("buffer_load_dwordx4 %0, %1, %2 offen offset:%3 lds" ::"v"(v), "s"(d.srd), "s"(so), "i"(1024 * J));
  }
}
DI void dma_all(const Dma& d) { dma_piece<0>(d); dma_piece<1>(d); dma_piece<2>(d); dma_piece<3>(d); }
template <int K0, int KS> DI void dma_slot(const Dma& d, int k) {
  if (k == K0) dma_piece<0>(d);
  if (k == K0 + KS) dma_piece<1>(d);
  if (k == K0 + 2 * KS) dma_piece<2>(d);
  if (k == K0 + 3 * KS) dma_piece<3>(d);
}
struct NoPre { DI void operator()() const {} };
template <int N, int D, int A, class RD, class MP, class MC, class FL, class PR = NoPre>
DI void pstep(RD rd, MP mprev, MC mcur, FL fill, PR pre = PR()) {
#pragma unroll
  for (int k = 0; k < N; ++k) {
#pragma unroll
    for (int h = 0; h < 2; ++h) {
      if (k < D) mprev(N - D + k, h); else mcur(k - D, h);
      SBAR();
      if (k == 0) {
        if (h == 0) pre();
#pragma unroll
        for (int q = h; q < A; q += 2) { rd(q, 0); rd(q, 1); }
      }
      if (k + A < N) rd(k + A, h);
      fill(k, h);
      SBAR();
    }
  }
}

__global__ __launch_bounds__(512, 2) void mha_fused(const char* __restrict__ xA, const char* __restrict__ kA, const char* __restrict__ vA,
                                                    const char* __restrict__ wqA, const char* __restrict__ woA, const char* __restrict__ xB,
                                                    const char* __restrict__ kB, const char* __restrict__ vB, const char* __restrict__ wqB,
                                                    const char* __restrict__ woB, char* __restrict__ ws, char* __restrict__ out) {
  extern __shared__ __attribute__((aligned(16))) char lds[];
  unsigned lane = threadIdx.x & 63;
  const unsigned tid = threadIdx.x, wid = rfl(tid >> 6), role = wid >> 1, qh = wid & 1;
  const unsigned xg = blockIdx.x & 7, p = xg >> 2, hg = xg & 3, b = blockIdx.x >> 3;
  const unsigned rot = b >> 3;
  const char* kc = (p ? kB : kA) + (size_t)(b * 32 + hg * 8) * 131072;
  const char* vc = (p ? vB : vA) + (size_t)(b * 32 + hg * 8) * 131072;
  const char* wq = p ? wqB : wqA;
  const char* wo = p ? woB : woA;
  const char* xp = (p ? xB : xA) + (size_t)b * 65536;
  const rsrc_t sK = __builtin_amdgcn_make_buffer_rsrc((void*)kc, (short)0, 1 << 20, 0x00020000);
  const rsrc_t sV = __builtin_amdgcn_make_buffer_rsrc((void*)vc, (short)0, 1 << 20, 0x00020000);
  const rsrc_t sQ = __builtin_amdgcn_make_buffer_rsrc((void*)wq, (short)0, 1 << 22, 0x00020000);
  const rsrc_t sO = __builtin_amdgcn_make_buffer_rsrc((void*)wo, (short)0, 1 << 22, 0x00020000);
  const rsrc_t sX = __builtin_amdgcn_make_buffer_rsrc((void*)xp, (short)0, 65536, 0x00020000);
  const unsigned ldsb = (unsigned)(size_t)lds;
  unsigned g, r, dq, dO, dV, dK, tq, tbh, xb;
  auto setup = [&](bool opaque) {
    if (opaque) asm volatile("v_mbcnt_lo_u32_b32 %0, -1, 0\n\tv_mbcnt_hi_u32_b32 %0, -1, %0" : "=v"(lane));
    g = lane >> 4; r = lane & 15;
    const unsigned swT = 16 * (r ^ (2 * g));
    dq = g * 8192 + swT; dO = g * 1024 + swT; dV = g * 256 + swT; dK = g * 256 + 16 * (r ^ g);
    const unsigned q4 = r >> 2;
    tq = ldsb + (8 * g + q4) * 256 + 32 * (q4 | ((g & 1) << 2)) + 8 * (lane & 3);
    tbh = ldsb + 2 * REG + (lane >> 2) * 256 + 32 * ((lane >> 2) & 7) + 8 * (lane & 3);
    xb = ldsb + XB + qh * 10240 + lane * 16;
    if (opaque) { PIN(tbh); PIN(xb); }
  };
  setup(false);
#pragma unroll
  for (int i = 0; i < 8; ++i) {
    const unsigned m0 = ldsb + role * REG + (1 + qh) * SLOT + i * 1024, so = (16 * role + 8 * qh + i) * 1024;
    const unsigned v = 16 * (((lane ^ (8 * qh + i)) + 16 * rot) & 63);
    asm volatile("s_mov_b32 m0, %0\n\ts_nop 0\n\tbuffer_load_dwordx4 %1, %2, %3 offen lds" ::"s"(m0), "v"(v), "s"(sX), "s"(so) : "m0");
  }
  const unsigned mq = ldsb + qh * 4096;
  auto head = [&](unsigned u) -> unsigned { return (u + b) & 7u; };
  auto mkWq = [&](unsigned u, unsigned i, unsigned reg, unsigned slot, bool nt = false) -> Dma {
    return Dma{sQ, mq + reg * REG + slot * SLOT, (32 * ((i + 4 * rot) & 15) + 16 * qh) * 8192 + (8 * hg + head(u)) * 256, 4 * 8192 - 1024, dq, nt, 2};
  };
  auto mkWo = [&](unsigned u, unsigned dblk, unsigned nq, unsigned reg, unsigned slot) -> Dma {
    return Dma{sO, mq + reg * REG + slot * SLOT, ((8 * hg + head(u)) * 128 + 32 * dblk + 16 * qh) * 1024 + ((nq + rot + hg) & 3) * 256, 4 * 1024 - 1024, dO, false, 0};
  };
  auto stK = [&](int sg) -> Dma {
    const bool ok = (unsigned)sg < 128u;
    return Dma{sK, ok ? mq + REG + (sg & 3) * SLOT : NOLDS, ok ? head(sg >> 4) * 131072 + (sg & 15) * 8192 + qh * 4096 : OOR, 0, dK, true, 1};
  };
  auto stV = [&](int sg) -> Dma {
    const int t = sg - 3; const bool ok = (unsigned)t < 128u;
    return Dma{sV, ok ? mq + 2 * REG + (sg & 3) * SLOT : NOLDS, ok ? head(t >> 4) * 131072 + (t & 15) * 8192 + qh * 4096 : OOR, 0, dV, true, 0};
  };
  auto stQ = [&](int sg) -> Dma {
    const int t = sg + 17; const bool ok = t < 128;
    Dma d = mkWq(t >> 4, t & 15, 0, sg & 3);
    d.m0 = ok ? d.m0 : NOLDS; d.so = ok ? d.so : OOR;
    return d;
  };
  auto stO = [&](int sg) -> Dma {
    const int t = sg - 20;
    Dma d = mkWo(t >> 4, (t >> 2) & 3, t & 3, 3, sg & 3);
    d.m0 = t < 112 ? d.m0 : NOLDS; d.so = (unsigned)t < 112u ? d.so : OOR;
    return d;
  };
  asm volatile("s_barrier" ::: "memory");
  dma_all(mkWq(0, role, role, 3, true));
  s16x4 fh[10][2];
  auto FA = [&](int f) { return cat(fh[f][0], fh[f][1]); };
  auto rdT = [&](int f, int h, unsigned tb, unsigned off, int ct) { fh[f][h] = trd((tb ^ (32u * ct)) + (off + 4096 * h)); };
  auto rdQ = [&](int f, int h, unsigned tb, unsigned off, int ct) { fh[f][h] = trd((tb ^ (32u * ct)) + (off + 1024 * h)); };
  auto nop = [&](int, int) {};
  auto zfa = [&]() {
#pragma unroll
    for (int i = 0; i < 10; ++i) fh[i][0] = fh[i][1] = (s16x4){0, 0, 0, 0};
  };
  auto prologue = [&](bf16x8 (*xf)[2], const int R, auto extra) {
    waitbar<4>();
    {
      const Dma f4 = mkWq(0, 4 + R, R, 0);
      const unsigned xa = ldsb + 2 * qh * REG + (1 + (r >> 3)) * SLOT + (r & 7) * 1024 + 16 * (g ^ r);
#pragma unroll
      for (int i = 0; i < 16; ++i) {
        xf[i][0] = ldsr128((xa ^ (64u * (i & 3))) + 256 * (i >> 2));
        xf[i][1] = ldsr128((xa ^ (64u * (i & 3))) + 256 * (i >> 2) + REG);
        dma_slot<3, 4>(f4, i);
      }
    }
    waitbar<4>();
    f32x4 q0[2][2] = {{zero4(), zero4()}, {zero4(), zero4()}};
    unsigned tqh = tq + 2 * REG;
    PIN(tqh);
    auto mm0 = [&](int j, int m, int t) { q0[m & 1][t] = mfma(FA(m), xf[4 * j + (m >> 1)][t], q0[m & 1][t]); };
#pragma unroll
    for (int j = 0; j < 4; ++j) {
      if (j == 0) { dma_all(mkWq(0, 8 + R, R, 1)); dma_all(mkWq(0, 12 + R, R, 2)); }
      if (j >= 1) { const int sg = j - 2; dma_all(R == 0 ? stQ(sg) : R == 1 ? stK(sg) : R == 2 ? stV(sg) : stO(sg)); }
      pstep<8, 3, 2>([&](int m, int h) { rdQ(m, h, m < 4 ? tq : tqh, ((m >> 1) & 1) * REG + ((j + 3) & 3) * SLOT, 2 * R + (m & 1)); },
                     [&](int m, int t) { if (j > 0) mm0(j - 1, m, t); }, [&](int m, int t) { mm0(j, m, t); }, nop);
      if (j == 0) extra();
      if (R == 2) { if (j < 3) waitbar<25>(); else bar(); } else waitbar<8>();
    }
#pragma unroll
    for (int m = 10; m < 16; ++m) mm0(3, m >> 1, m & 1);
    ldsw128(xb + 2048 * R + (R == 3 ? 2048 : 0), swap16(pack8(q0[0][0] * SC, q0[1][0] * SC)));
    ldsw128(xb + 2048 * R + (R == 3 ? 2048 : 0) + 1024, swap16(pack8(q0[0][1] * SC, q0[1][1] * SC)));
    SBAR();
  };
  auto noextra = [&]() {};
  const rsrc_t sP = __builtin_amdgcn_make_buffer_rsrc((void*)ws, (short)0, WS_FLAG, 0x00020000);

  if (role == 0) {
    setup(true);
    bf16x8 xf[16][2];
    prologue(xf, 0, noextra);
    f32x4 qacc[8][2];
#pragma unroll
    for (int i = 0; i < 8; ++i) qacc[i][0] = qacc[i][1] = zero4();
    auto mmQ = [&](int i, int m, int t) { qacc[m][t] = mfma(FA(m), xf[i][t], i == 0 ? zero4() : qacc[m][t]); };
    auto putQ = [&](int k) {
      ldsw128(xb + 1024 * (k < 6 ? k : k + 2), swap16(pack8(qacc[k & 6][k & 1] * SC, qacc[(k & 6) + 1][k & 1] * SC)));
    };
    for (int u = 1; u < 8; ++u) {
#pragma unroll
      for (int i = 0; i < 16; ++i) {
        const Dma da = stQ(16 * (u - 1) + i + 2);
        pstep<8, D1, AH>([&](int m, int h) { rdQ(m, h, tq, ((i + 3) & 3) * SLOT, m); },
                         [&](int m, int t) { mmQ((i + 15) & 15, m, t); }, [&](int m, int t) { mmQ(i, m, t); },
                         [&](int k, int h) { if (h == 0 && k == 0) dma_all(da); if (h == 1 && i == 0 && u > 1) putQ(k); });
        waitbar<8>();
      }
    }
#pragma unroll
    for (int m = 8 - D1; m < 8; ++m) { mmQ(15, m, 0); mmQ(15, m, 1); }
#pragma unroll
    for (int k = 0; k < 8; ++k) putQ(k);
    bar();
#pragma unroll
    for (int j = 0; j < 4; ++j) { dma_all(mkWo(7, 0, j + 1, 0, j)); bar(); bar(); bar(); bar(); }
    dma_all(mkWo(7, 1, 1, 1, 0)); dma_all(mkWo(7, 1, 2, 1, 1)); bar();
    dma_all(mkWo(7, 2, 1, 2, 0)); dma_all(mkWo(7, 3, 1, 3, 0)); dma_all(mkWo(7, 1, 3, 1, 2)); bar();
    dma_all(mkWo(7, 2, 2, 2, 1)); dma_all(mkWo(7, 3, 2, 3, 1)); dma_all(mkWo(7, 1, 0, 1, 3)); bar();
    dma_all(mkWo(7, 2, 3, 2, 2)); dma_all(mkWo(7, 3, 3, 3, 2)); dma_all(mkWo(7, 2, 0, 2, 3)); waitbar<28>();
    dma_all(mkWo(7, 3, 0, 3, 3)); waitbar<20>();
    waitbar<8>();
    waitbar<0>();
    bar();
  } else if (role == 1) {
    setup(true);
    { bf16x8 xf[16][2]; prologue(xf, 1, noextra); }
    dma_all(stK(2));
    const unsigned sig = ((g & 1) << 1) | (g >> 1);
    const unsigned kb = ldsb + REG + r * 256 + 16 * (sig ^ r);
    unsigned pb = ldsb + PB + qh * 3072 + lane * 16;
    PIN(pb);
    f32x4 sb[4][2][2], negm[2] = {zero4(), zero4()};
    bf16x8 qf[4][2], fa[8];
#pragma unroll
    for (int i = 0; i < 16; ++i) sb[i >> 2][(i >> 1) & 1][i & 1] = zero4();
#pragma unroll
    for (int i = 0; i < 8; ++i) { qf[i >> 1][i & 1] = zero8(); fa[i] = zero8(); }
    float mx[2] = {0.f, 0.f}, mc = 0.f;
    unsigned flag = 0;
    auto mmK = [&](int i, int m, int t) {
      sb[i & 3][m >> 2][t] = mfma(fa[m], qf[m & 3][t], (m & 3) == 0 ? (i < 2 ? zero4() : negm[t]) : sb[i & 3][m >> 2][t]);
    };
    auto soft = [&](int i, int k, int h, bool chk) {
      const int je = (i + 14) & 15, jm = (i + 15) & 15;
      auto& se = sb[je & 3];
      auto& sm = sb[jm & 3];
      auto g0 = [](int q) { return q < 4 ? 3 * q : 12 + 2 * (q - 4); };
      auto gn = [](int q) { return q < 4 ? 3 : 2; };
      if (h == 0 && k < 6) {
#pragma unroll
        for (int e = g0(k); e < g0(k) + gn(k); ++e) se[(e >> 2) & 1][e >> 3][e & 3] = __builtin_amdgcn_exp2f(se[(e >> 2) & 1][e >> 3][e & 3]);
      }
      if (h == 0) return;
      if (k == 3) { u32x4 w = pack8(se[0][0], se[1][0]); w[0] |= flag; ldsw128(pb + (je & 1) * 6144, w); }
      if (k == 6) ldsw128(pb + (je & 1) * 6144 + 1024, pack8(se[0][1], se[1][1]));
      if (chk) {
        if (k == 3) { mx[0] = max3f(sm[0][0][0], sm[0][0][1], sm[0][0][2]); mx[1] = max3f(sm[0][1][0], sm[0][1][1], sm[0][1][2]); PIN(mx[0]); PIN(mx[1]); }
        if (k == 4) { mx[0] = max3f(mx[0], sm[0][0][3], sm[1][0][0]); mx[1] = max3f(mx[1], sm[0][1][3], sm[1][1][0]); PIN(mx[0]); PIN(mx[1]); }
        if (k == 5) { mx[0] = max3f(mx[0], sm[1][0][1], sm[1][0][2]); mx[1] = max3f(mx[1], sm[1][1][1], sm[1][1][2]); PIN(mx[0]); PIN(mx[1]); }
        if (k == 6 && jm != 0) { mc = max3f(sm[1][0][3], sm[1][1][3], mx[0]); PIN(mc); mc = max3f(mc, mx[1], 0.f); }
        if (k == 7) {
          flag = 0;
          if (jm == 0) {
#pragma unroll
            for (int t = 0; t < 2; ++t) {
              const float d = red_max(__builtin_fmaxf(mx[t], sm[1][t][3]));
              sm[0][t] -= d; sm[1][t] -= d; sb[i & 3][0][t] -= d; sb[i & 3][1][t] -= d;
              negm[t] = (f32x4){-d, -d, -d, -d};
            }
          } else if (__builtin_expect(__builtin_amdgcn_ballot_w64(mc > THR) != 0, 0)) {
            flag = 0x80000000u;
            f32x2 al;
#pragma unroll
            for (int t = 0; t < 2; ++t) {
              const float d = __builtin_fmaxf(red_max(__builtin_fmaxf(mx[t], sm[1][t][3])), 0.f);
              al[t] = __builtin_amdgcn_exp2f(-d);
              sm[0][t] -= d; sm[1][t] -= d;
              if (jm != 15) { sb[i & 3][0][t] -= d; sb[i & 3][1][t] -= d; negm[t] -= d; }
            }
            *(LAS f32x2*)(size_t)(pb + (jm & 1) * 6144 + 2048 + 8 * ((jm >> 1) & 1)) = al;
          }
        }
      }
    };
#pragma unroll
    for (int i = 0; i < 16; ++i) PIN(sb[i >> 2][(i >> 1) & 1][i & 1]);
#pragma unroll
    for (int i = 0; i < 8; ++i) { PIN(qf[i >> 1][i & 1]); PIN(fa[i]); }
    waitbar<8>();
    for (int u = 0; u < 8; ++u) {
#pragma unroll
      for (int i = 0; i < 16; ++i) {
        const Dma db = stK(16 * u + i + 3);
        pstep<8, 3, AH>([&](int m, int h) { if (h == 0) fa[m] = ldsr128((kb ^ (64u * (m & 3))) + ((i & 3) * SLOT + 4096 * (m >> 2))); },
                        [&](int m, int t) { mmK((i + 15) & 15, m, t); }, [&](int m, int t) { mmK(i, m, t); },
                        [&](int k, int h) {
                          if (h == 1 && i == 0 && k < 3) {
                            qf[k + 1][0] = ldsr128(xb + 2048 * (k < 2 ? k + 1 : 4)); qf[k + 1][1] = ldsr128(xb + 2048 * (k < 2 ? k + 1 : 4) + 1024);
                          }
                          if (h == 1 && k == 2) dma_all(db);
                          soft(i, k, h, true);
                        },
                        [&]() { if (i == 0) { qf[0][0] = ldsr128(xb); qf[0][1] = ldsr128(xb + 1024); } });
        waitbar<8>();
      }
    }
#pragma unroll
    for (int m = 10; m < 16; ++m) mmK(15, m >> 1, m & 1);
#pragma unroll
    for (int k = 0; k < 16; ++k) soft(0, k >> 1, k & 1, true);
    bar();
#pragma unroll
    for (int k = 0; k < 16; ++k) soft(1, k >> 1, k & 1, false);
    bar();
    bar(); bar(); bar(); bar(); bar();
    bar();
  } else {
    const unsigned grp = blockIdx.x & ~3u;
    auto stP = [&](unsigned ln, f32x4 a0, f32x4 a1, int gi, int ct) {
      const unsigned so = ((grp + ((hg + gi) & 3)) * 3 + gi - 1) * 16384 + qh * 8192 + (ct >> 2) * 4096;
      __builtin_amdgcn_raw_buffer_store_b128(pack8(a0, a1), sP, ln * 16 + (ct & 3) * 1024, so, 16);
    };
    auto putF = [&](unsigned ln, f32x4 a, int n) { ldsw128(ldsb + (n >> 2) * REG + qh * SLOT + (n & 3) * 1024 + ln * 16, __builtin_bit_cast(u32x4, a)); };
    if (role == 2) {
      setup(true);
#pragma unroll
      for (int i = 0; i < 10; ++i) ldsw128(xb + 1024 * i, (u32x4){0, 0, 0, 0});
      GAS unsigned long long* ep = (GAS unsigned long long*)(ws + WS_EPOCH + (blockIdx.x >> 2) * 128);
      unsigned long long tag;
      u32x4 wm[16];
      {
        bf16x8 xf[16][2];
        prologue(xf, 2, [&]() {
          asm volatile("global_load_dwordx2 %0, %1, off sc1" : "=&v"(tag) : "v"(ep) : "memory");
#pragma unroll
          for (int i = 0; i < 16; ++i) {
            const unsigned so = ((8 * hg + head(0)) * 128 + 64 * qh + 4 * i) * 1024 + ((rot + hg) & 3) * 256;
            asm volatile("buffer_load_dwordx4 %0, %1, %2, %3 offen" : "=&v"(wm[i]) : "v"(dO), "s"(sO), "s"(so) : "memory");
          }
        });
      }
      bar();
      dma_all(stV(3)); bar();
      dma_all(stV(4)); bar();
      dma_all(stV(5));
      asm volatile("s_waitcnt vmcnt(8) lgkmcnt(0)\n\ts_barrier"
                   : "+v"(tag), "+v"(wm[0]), "+v"(wm[1]), "+v"(wm[2]), "+v"(wm[3]), "+v"(wm[4]), "+v"(wm[5]), "+v"(wm[6]), "+v"(wm[7]), "+v"(wm[8]), "+v"(wm[9]),
                     "+v"(wm[10]), "+v"(wm[11]), "+v"(wm[12]), "+v"(wm[13]), "+v"(wm[14]), "+v"(wm[15])::"memory");
      tag = ((unsigned long long)rfl((unsigned)(tag >> 32)) << 32) | rfl((unsigned)tag);
      tag = (tag ^ (tag >> 31)) * 0x9E3779B97F4A7C15ull + 0x632BE59BD9B4E019ull;
      unsigned pb = ldsb + PB + qh * 3072 + lane * 16;
      PIN(pb);
      f32x4 o[8][2], oa[4][2][2];
      bf16x8 pf[2][2], of[2][2];
#pragma unroll
      for (int i = 0; i < 16; ++i) { o[i >> 1][i & 1] = zero4(); oa[i >> 2][(i >> 1) & 1][i & 1] = zero4(); }
      zfa();
      pf[0][0] = pf[0][1] = pf[1][0] = pf[1][1] = of[0][0] = of[0][1] = of[1][0] = of[1][1] = zero8();
      f32x2 linv = {0.f, 0.f};
      f32x4 lacc[2] = {zero4(), zero4()};
      bf16x8 ones = {0x3f80, 0x3f80, 0x3f80, 0x3f80, 0x3f80, 0x3f80, 0x3f80, 0x3f80};
      PIN(ones);
      auto mm3 = [&](int j, int m, int t) {
        if (m < 8) o[m][t] = mfma(FA(m), pf[j & 1][t], j == 0 ? zero4() : o[m][t]);
        else { const int iw = (j + 15) & 15; oa[iw & 3][m & 1][t] = mfma(FA(m), of[(iw >> 2) & 1][t], oa[iw & 3][m & 1][t]); }
      };
      auto packO = [&](int k) { return pack8(o[k & 6][k & 1] * linv[k & 1], o[(k & 6) + 1][k & 1] * linv[k & 1]); };
      for (int u = 0; u < 8; ++u) {
#pragma unroll
        for (int j = 0; j < 16; ++j) {
          const int iw = (j + 15) & 15;
          const Dma da = stV(16 * u + 6 + j);
          pstep<10, D3, AH>([&](int m, int h) { if (m < 8) rdT(m, h, tbh, ((j + 3) & 3) * SLOT, m); else rdT(m, h, tbh, REG + ((j + 3) & 3) * SLOT, m - 2); },
                            [&](int m, int t) { mm3((j + 15) & 15, m, t); }, [&](int m, int t) { mm3(j, m, t); },
                            [&](int k, int h) {
                              if (h == 0) { if (k == 5) dma_all(da); return; }
                              if (j == 0 && k < 8) ldsw128(xb + 1024 * k, packO(k));
                              if (j > 0 && k == D3 - 1) {
                                if (__builtin_expect(__builtin_amdgcn_ballot_w64((int)__builtin_bit_cast(u32x4, pf[j & 1][0])[0] < 0) != 0, 0)) {
                                  const f32x2 alpha = *(const LAS f32x2*)(size_t)(pb + (j & 1) * 6144 + 2048 + 8 * ((j >> 1) & 1));
                                  { u32x4 w = __builtin_bit_cast(u32x4, pf[j & 1][0]); w[0] &= 0x7fffffffu; pf[j & 1][0] = __builtin_bit_cast(bf16x8, w); }
                                  f32x4 a0 = {alpha[0], alpha[0], alpha[0], alpha[0]}, a1 = {alpha[1], alpha[1], alpha[1], alpha[1]};
                                  PIN(a0); PIN(a1);
#pragma unroll
                                  for (int ct = 0; ct < 8; ++ct) { o[ct][0] *= a0; o[ct][1] *= a1; }
                                  lacc[0] *= a0; lacc[1] *= a1;
                                }
                              }
                              if (k == 4 || k == 6) lacc[k == 6] = mfma(ones, pf[j & 1][k == 6], j == 0 ? zero4() : lacc[k == 6]);
                              if (j == 15 && k == 9) linv = (f32x2){1.0f / lacc[0][0], 1.0f / lacc[1][0]};
                              if (k == 8 && (iw & 3) == 0) { of[(iw >> 2) & 1][0] = ldsr128(xb + 2048 * (iw >> 2)); of[(iw >> 2) & 1][1] = ldsr128(xb + 2048 * (iw >> 2) + 1024); }
                            },
                            [&]() {
                              pf[j & 1][0] = ldsr128(pb + (j & 1) * 6144); pf[j & 1][1] = ldsr128(pb + (j & 1) * 6144 + 1024);
                            });
          waitbar<8>();
        }
      }
#pragma unroll
      for (int m = 10 - D3; m < 10; ++m) { mm3(15, m, 0); mm3(15, m, 1); }
      rdT(8, 0, tbh, REG + 3 * SLOT, 6); rdT(8, 1, tbh, REG + 3 * SLOT, 6); rdT(9, 0, tbh, REG + 3 * SLOT, 7); rdT(9, 1, tbh, REG + 3 * SLOT, 7);
      bf16x8 og[8];
#pragma unroll
      for (int k = 0; k < 8; ++k) { const u32x4 w = packO(k); ldsw128(xb + 1024 * k, w); og[k] = __builtin_bit_cast(bf16x8, w); }
      mm3(0, 8, 0); mm3(0, 8, 1); mm3(0, 9, 0); mm3(0, 9, 1);
      bar();
      unsigned ln;
      asm volatile("v_mbcnt_lo_u32_b32 %0, -1, 0\n\tv_mbcnt_hi_u32_b32 %0, -1, %0" : "=v"(ln));
      const unsigned tl2 = tbh - 2 * REG;
#pragma unroll
      for (int j = 0; j < 4; ++j) {
        const int gi = (j + 1) & 3;
#pragma unroll
        for (int m = 0; m < 16; ++m) rdT(m >> 1, m & 1, m < 8 ? tl2 : tbh, ((m >> 2) & 1) * REG + j * SLOT, 6 + ((m >> 1) & 1));
#pragma unroll
        for (int m = 0; m < 8; ++m) {
          oa[gi][m & 1][0] = mfma(FA(m), og[m & 6], oa[gi][m & 1][0]);
          oa[gi][m & 1][1] = mfma(FA(m), og[(m & 6) + 1], oa[gi][m & 1][1]);
        }
        if (j < 3) {
          stP(ln, oa[gi][0][0], oa[gi][0][1], gi, 6); stP(ln, oa[gi][1][0], oa[gi][1][1], gi, 7);
          bar();
        }
      }
#pragma unroll
      for (int q = 0; q < 4; ++q) putF(ln, oa[0][q >> 1][q & 1], 12 + q);
      waitbar<0>();
      if (qh == 0) {
        if (ln < 3)
          __hip_atomic_store((GAS unsigned long long*)(ws + WS_FLAG + ((grp + ((hg + ln + 1) & 3)) * 4 + ln) * 128), tag, __ATOMIC_RELAXED, __HIP_MEMORY_SCOPE_AGENT);
        GAS unsigned long long* fl = (GAS unsigned long long*)(ws + WS_FLAG + (blockIdx.x * 4 + (ln < 3 ? ln : 0)) * 128);
        for (unsigned spins = 0; spins < (1u << 22); ++spins) {
          const unsigned long long v = __hip_atomic_load(fl, __ATOMIC_RELAXED, __HIP_MEMORY_SCOPE_AGENT);
          if (__all(v == tag)) break;
          __builtin_amdgcn_s_sleep(1);
        }
        if (hg == 0 && ln == 0) __hip_atomic_store(ep, tag, __ATOMIC_RELAXED, __HIP_MEMORY_SCOPE_AGENT);
      }
    } else {
      setup(true);
      { bf16x8 xf[16][2]; prologue(xf, 3, noextra); }
      dma_all(stO(2)); waitbar<8>();
      for (int s = 0; s < 17; ++s) waitbar<0>();
      dma_all(stO(20)); bar(); dma_all(stO(21)); bar(); dma_all(stO(22)); waitbar<8>();
      f32x4 oa[4][6][2];
      bf16x8 of[2][2];
#pragma unroll
      for (int i = 0; i < 48; ++i) oa[i / 12][(i % 12) >> 1][i & 1] = zero4();
      zfa();
      of[0][0] = of[0][1] = of[1][0] = of[1][1] = zero8();
      auto mm4 = [&](int nq, int par, int m, int t) { oa[nq][m][t] = mfma(FA(m), of[par][t], oa[nq][m][t]); };
      for (int u = 0; u < 7; ++u) {
#pragma unroll
        for (int i = 0; i < 16; ++i) {
          const int ip = (i + 15) & 15;
          const Dma db = stO(16 * u + 23 + i);
          pstep<6, D4, AH>([&](int m, int h) { rdT(m, h, tbh, REG + (i & 3) * SLOT, m); },
                           [&](int m, int t) { mm4(ip & 3, (ip >> 2) & 1, m, t); }, [&](int m, int t) { mm4(i & 3, (i >> 2) & 1, m, t); },
                           [&](int k, int h) { if (h == 0 && k == 2) dma_all(db); },
                           [&]() { if ((i & 3) == 0) { of[(i >> 2) & 1][0] = ldsr128(xb + 2048 * (i >> 2)); of[(i >> 2) & 1][1] = ldsr128(xb + 2048 * (i >> 2) + 1024); } });
          waitbar<8>();
        }
      }
      unsigned ln;
      asm volatile("v_mbcnt_lo_u32_b32 %0, -1, 0\n\tv_mbcnt_hi_u32_b32 %0, -1, %0" : "=v"(ln));
      const unsigned tl2 = tbh - 2 * REG;
#pragma unroll
      for (int j = 0; j < 4; ++j) {
        const int gi = (j + 1) & 3;
#pragma unroll
        for (int q = 0; q < 4; ++q) {
          pstep<6, D4, AH>([&](int m, int h) { rdT(m, h, q < 2 ? tl2 : tbh, (q & 1) * REG + j * SLOT, m); },
                           [&](int m, int t) { if (q > 0) mm4(gi, (q - 1) & 1, m, t); else mm4(j == 0 ? 3 : j, 1, m, t); },
                           [&](int m, int t) { mm4(gi, q & 1, m, t); },
                           [&](int k, int h) {
                             if (h == 1 && j > 0 && q == 0) stP(ln, oa[j][k][0], oa[j][k][1], j, k);
                           },
                           [&]() { of[q & 1][0] = ldsr128(xb + 2048 * q); of[q & 1][1] = ldsr128(xb + 2048 * q + 1024); });
          if (j == 3 && q == 1) waitbar<0>();
        }
        if (j < 3) bar();
      }
#pragma unroll
      for (int m = 6 - D4; m < 6; ++m) { mm4(0, 1, m, 0); mm4(0, 1, m, 1); }
#pragma unroll
      for (int n = 0; n < 12; ++n) putF(ln, oa[0][n >> 1][n & 1], n);
    }
  }
  waitbar<0>();
  {
    unsigned ln;
    asm volatile("v_mbcnt_lo_u32_b32 %0, -1, 0\n\tv_mbcnt_hi_u32_b32 %0, -1, %0" : "=v"(ln));
    f32x4 a[4];
    u32x4 in[6];
#pragma unroll
    for (int j = 0; j < 3; ++j)
#pragma unroll
      for (int c = 0; c < 2; ++c)
        in[2 * j + c] = __builtin_amdgcn_raw_buffer_load_b128(sP, ln * 16 + c * 1024, (blockIdx.x * 3 + j) * 16384 + qh * 8192 + role * 2048, 16);
#pragma unroll
    for (int q = 0; q < 4; ++q) {
      a[q] = __builtin_bit_cast(f32x4, ldsr128(ldsb + role * REG + qh * SLOT + q * 1024 + ln * 16));
#pragma unroll
      for (int j = 0; j < 3; ++j) {
        const unsigned w0 = in[2 * j + (q >> 1)][2 * (q & 1)], w1 = in[2 * j + (q >> 1)][2 * (q & 1) + 1];
        a[q] += (f32x4){__uint_as_float(w0 << 16), __uint_as_float(w0 & 0xffff0000u), __uint_as_float(w1 << 16), __uint_as_float(w1 & 0xffff0000u)};
      }
    }
    char* orow = out + ((size_t)b * 64 + 32 * qh + (ln & 15)) * 2048 + p * 1024 + ((rot + hg) & 3) * 256 + role * 64 + ((ln & 16) * 2 + (ln >> 5) * 16);
    *(u32x4*)(orow) = swap16(pack8(a[0], a[2]));
    *(u32x4*)(orow + 16 * 2048) = swap16(pack8(a[1], a[3]));
  }
}

extern "C" void kernel_launch(void* const* d_in, const int* in_sizes, int n_in, void* d_out, int out_size, void* d_ws, size_t ws_size,
                              hipStream_t stream) {
  (void)in_sizes; (void)n_in; (void)out_size; (void)ws_size;
  (void)hipFuncSetAttribute((const void*)mha_fused, hipFuncAttributeMaxDynamicSharedMemorySize, LDS_BYTES);
  mha_fused<<<256, 512, LDS_BYTES, stream>>>((const char*)d_in[0], (const char*)d_in[1], (const char*)d_in[2], (const char*)d_in[3],
                                             (const char*)d_in[4], (const char*)d_in[5], (const char*)d_in[6], (const char*)d_in[7],
                                             (const char*)d_in[8], (const char*)d_in[9], (char*)d_ws, (char*)d_out);
}
```

```cpp
#include <hip/hip_runtime.h>
#include <hip/hip_bf16.h>

typedef __amdgpu_buffer_rsrc_t rsrc_t;
using bf16x8 = __attribute__((ext_vector_type(8))) short;
using s16x4 = __attribute__((ext_vector_type(4))) short;
using f32x4 = __attribute__((ext_vector_type(4))) float;
using f32x2 = __attribute__((ext_vector_type(2))) float;
using u32x4 = __attribute__((ext_vector_type(4))) unsigned;
using bf16x2_t = __attribute__((ext_vector_type(2))) __bf16;
typedef short v4i16_t __attribute__((ext_vector_type(4)));
#define LAS __attribute__((address_space(3)))
#define GAS __attribute__((address_space(1)))
#define DI __device__ __forceinline__
#define SBAR() __builtin_amdgcn_sched_barrier(0)
#define PIN(x) asm volatile("" : "+v"(x))

constexpr int SLOT = 8192, REG = 32768;
constexpr int XB = 4 * REG;
constexpr int PB = XB + 2 * 10240;
constexpr int LDS_BYTES = PB + 4 * 3072;
constexpr unsigned WS_FLAG = 32u << 20, WS_EPOCH = 33u << 20;
constexpr float SC = 0.08838834764831845f * 1.4426950408889634f;
constexpr unsigned OOR = 0x80000000u;
constexpr unsigned NOLDS = 0x30000u;
#define THR 24.0f
constexpr int D1 = 3, D3 = 3, D4 = 2, AH = 2;

DI unsigned rfl(unsigned x) { return __builtin_amdgcn_readfirstlane(x); }
DI unsigned cvtpk(float lo, float hi) { f32x2 v = {lo, hi}; return __builtin_bit_cast(unsigned, __builtin_convertvector(v, bf16x2_t)); }
DI f32x4 mfma(bf16x8 a, bf16x8 b, f32x4 c) { return __builtin_amdgcn_mfma_f32_16x16x32_bf16(a, b, c, 0, 0, 0); }
DI s16x4 trd(unsigned addr) { return __builtin_bit_cast(s16x4, __builtin_amdgcn_ds_read_tr16_b64_v4i16((LAS v4i16_t*)(size_t)addr)); }
DI bf16x8 ldsr128(unsigned addr) { return *(const LAS bf16x8*)(size_t)addr; }
DI void ldsw128(unsigned addr, u32x4 v) { *(LAS u32x4*)(size_t)addr = v; }
DI bf16x8 cat(s16x4 a, s16x4 b) { return (bf16x8){a[0], a[1], a[2], a[3], b[0], b[1], b[2], b[3]}; }
DI f32x4 zero4() { return (f32x4){0.f, 0.f, 0.f, 0.f}; }
DI bf16x8 zero8() { return (bf16x8){0, 0, 0, 0, 0, 0, 0, 0}; }
DI u32x4 pack8(f32x4 a, f32x4 b) { return (u32x4){cvtpk(a[0], a[1]), cvtpk(a[2], a[3]), cvtpk(b[0], b[1]), cvtpk(b[2], b[3])}; }
DI u32x4 swap16(u32x4 w) {
  auto s0 = __builtin_amdgcn_permlane16_swap(w[0], w[2], false, false);
  auto s1 = __builtin_amdgcn_permlane16_swap(w[1], w[3], false, false);
  return (u32x4){s0[0], s1[0], s0[1], s1[1]};
}
DI float max3f(float a, float b, float c) { return __builtin_fmaxf(__builtin_fmaxf(a, b), c); }
DI float red_max(float v) {
  auto r = __builtin_amdgcn_permlane16_swap(__float_as_uint(v), __float_as_uint(v), false, false);
  v = __builtin_fmaxf(__uint_as_float(r[0]), __uint_as_float(r[1]));
  auto q = __builtin_amdgcn_permlane32_swap(__float_as_uint(v), __float_as_uint(v), false, false);
  return __builtin_fmaxf(__uint_as_float(q[0]), __uint_as_float(q[1]));
}
template <int N> DI void waitbar() { asm volatile("s_waitcnt vmcnt(%0) lgkmcnt(0)\n\ts_barrier" ::"i"(N) : "memory"); SBAR(); }
DI void bar() { asm volatile("s_waitcnt lgkmcnt(0)\n\ts_barrier" ::: "memory"); SBAR(); }

struct Dma { rsrc_t srd; unsigned m0, so, sstep, v0; bool nt; int kind; };
template <int J> DI void dma_piece(const Dma& d) {
  const unsigned v = d.v0 ^ (d.kind == 1 ? 64u * J : d.kind == 0 ? 128u * (J & 1) : 128u * (J >> 1));
  const unsigned so = d.so + J * d.sstep;
  if (J == 0) {
    if (d.nt) asm volatile("s_mov_b32 m0, %0\n\ts_nop 0\n\tbuffer_load_dwordx4 %1, %2, %3 offen offset:%4 nt lds" ::"s"(d.m0), "v"(v), "s"(d.srd), "s"(so), "i"(1024 * J) : "m0");
    else asm volatile("s_mov_b32 m0, %0\n\ts_nop 0\n\tbuffer_load_dwordx4 %1, %2, %3 offen offset:%4 lds" ::"s"(d.m0), "v"(v), "s"(d.srd), "s"(so), "i"(1024 * J) : "m0");
  } else {
    if (d.nt) asm volatile("buffer_load_dwordx4 %0, %1, %2 offen offset:%3 nt lds" ::"v"(v), "s"(d.srd), "s"(so), "i"(1024 * J));
    else asm volatile("buffer_load_dwordx4 %0, %1, %2 offen offset:%3 lds" ::"v"(v), "s"(d.srd), "s"(so), "i"(1024 * J));
  }
}
DI void dma_all(const Dma& d) { dma_piece<0>(d); dma_piece<1>(d); dma_piece<2>(d); dma_piece<3>(d); }
template <int K0, int KS> DI void dma_slot(const Dma& d, int k) {
  if (k == K0) dma_piece<0>(d);
  if (k == K0 + KS) dma_piece<1>(d);
  if (k == K0 + 2 * KS) dma_piece<2>(d);
  if (k == K0 + 3 * KS) dma_piece<3>(d);
}
struct NoPre { DI void operator()() const {} };
template <int N, int D, int A, class RD, class MP, class MC, class FL, class PR = NoPre>
DI void pstep(RD rd, MP mprev, MC mcur, FL fill, PR pre = PR()) {
#pragma unroll
  for (int k = 0; k < N; ++k) {
#pragma unroll
    for (int h = 0; h < 2; ++h) {
      if (k < D) mprev(N - D + k, h); else mcur(k - D, h);
      SBAR();
      if (k == 0) {
        if (h == 0) pre();
#pragma unroll
        for (int q = h; q < A; q += 2) { rd(q, 0); rd(q, 1); }
      }
      if (k + A < N) rd(k + A, h);
      fill(k, h);
      SBAR();
    }
  }
}

__global__ __launch_bounds__(512, 2) void mha_fused(const char* __restrict__ xA, const char* __restrict__ kA, const char* __restrict__ vA,
                                                    const char* __restrict__ wqA, const char* __restrict__ woA, const char* __restrict__ xB,
                                                    const char* __restrict__ kB, const char* __restrict__ vB, const char* __restrict__ wqB,
                                                    const char* __restrict__ woB, char* __restrict__ ws, char* __restrict__ out) {
  extern __shared__ __attribute__((aligned(16))) char lds[];
  unsigned lane = threadIdx.x & 63;
  const unsigned tid = threadIdx.x, wid = rfl(tid >> 6), role = wid >> 1, qh = wid & 1;
  const unsigned xg = blockIdx.x & 7, p = xg >> 2, hg = xg & 3, b = blockIdx.x >> 3;
  const unsigned rot = b >> 3;
  const char* kc = (p ? kB : kA) + (size_t)(b * 32 + hg * 8) * 131072;
  const char* vc = (p ? vB : vA) + (size_t)(b * 32 + hg * 8) * 131072;
  const char* wq = p ? wqB : wqA;
  const char* wo = p ? woB : woA;
  const char* xp = (p ? xB : xA) + (size_t)b * 65536;
  const rsrc_t sK = __builtin_amdgcn_make_buffer_rsrc((void*)kc, (short)0, 1 << 20, 0x00020000);
  const rsrc_t sV = __builtin_amdgcn_make_buffer_rsrc((void*)vc, (short)0, 1 << 20, 0x00020000);
  const rsrc_t sQ = __builtin_amdgcn_make_buffer_rsrc((void*)wq, (short)0, 1 << 22, 0x00020000);
  const rsrc_t sO = __builtin_amdgcn_make_buffer_rsrc((void*)wo, (short)0, 1 << 22, 0x00020000);
  const rsrc_t sX = __builtin_amdgcn_make_buffer_rsrc((void*)xp, (short)0, 65536, 0x00020000);
  const unsigned ldsb = (unsigned)(size_t)lds;
  unsigned g, r, dq, dO, dV, dK, tq, tbh, xb;
  auto setup = [&](bool opaque) {
    if (opaque) asm volatile("v_mbcnt_lo_u32_b32 %0, -1, 0\n\tv_mbcnt_hi_u32_b32 %0, -1, %0" : "=v"(lane));
    g = lane >> 4; r = lane & 15;
    const unsigned swT = 16 * (r ^ (2 * g));
    dq = g * 8192 + swT; dO = g * 1024 + swT; dV = g * 256 + swT; dK = g * 256 + 16 * (r ^ g);
    const unsigned q4 = r >> 2;
    tq = ldsb + (8 * g + q4) * 256 + 32 * (q4 | ((g & 1) << 2)) + 8 * (lane & 3);
    tbh = ldsb + 2 * REG + (lane >> 2) * 256 + 32 * ((lane >> 2) & 7) + 8 * (lane & 3);
    xb = ldsb + XB + qh * 10240 + lane * 16;
    if (opaque) { PIN(tbh); PIN(xb); }
  };
  setup(false);
#pragma unroll
  for (int i = 0; i < 8; ++i) {
    const unsigned m0 = ldsb + role * REG + (1 + qh) * SLOT + i * 1024, so = (16 * role + 8 * qh + i) * 1024;
    const unsigned v = 16 * (((lane ^ (8 * qh + i)) + 16 * rot) & 63);
    asm volatile("s_mov_b32 m0, %0\n\ts_nop 0\n\tbuffer_load_dwordx4 %1, %2, %3 offen lds" ::"s"(m0), "v"(v), "s"(sX), "s"(so) : "m0");
  }
  const unsigned mq = ldsb + qh * 4096;
  auto head = [&](unsigned u) -> unsigned { return (u + b) & 7u; };
  auto mkWq = [&](unsigned u, unsigned i, unsigned reg, unsigned slot, bool nt = false) -> Dma {
    return Dma{sQ, mq + reg * REG + slot * SLOT, (32 * ((i + 4 * rot) & 15) + 16 * qh) * 8192 + (8 * hg + head(u)) * 256, 4 * 8192 - 1024, dq, nt, 2};
  };
  auto mkWo = [&](unsigned u, unsigned dblk, unsigned nq, unsigned reg, unsigned slot) -> Dma {
    return Dma{sO, mq + reg * REG + slot * SLOT, ((8 * hg + head(u)) * 128 + 32 * dblk + 16 * qh) * 1024 + ((nq + rot + hg) & 3) * 256, 4 * 1024 - 1024, dO, false, 0};
  };
  auto stK = [&](int sg) -> Dma {
    const bool ok = (unsigned)sg < 128u;
    return Dma{sK, ok ? mq + REG + (sg & 3) * SLOT : NOLDS, ok ? head(sg >> 4) * 131072 + (sg & 15) * 8192 + qh * 4096 : OOR, 0, dK, true, 1};
  };
  auto stV = [&](int sg) -> Dma {
    const int t = sg - 3; const bool ok = (unsigned)t < 128u;
    return Dma{sV, ok ? mq + 2 * REG + (sg & 3) * SLOT : NOLDS, ok ? head(t >> 4) * 131072 + (t & 15) * 8192 + qh * 4096 : OOR, 0, dV, true, 0};
  };
  auto stQ = [&](int sg) -> Dma {
    const int t = sg + 17; const bool ok = t < 128;
    Dma d = mkWq(t >> 4, t & 15, 0, sg & 3);
    d.m0 = ok ? d.m0 : NOLDS; d.so = ok ? d.so : OOR;
    return d;
  };
  auto stO = [&](int sg) -> Dma {
    const int t = sg - 20;
    Dma d = mkWo(t >> 4, (t >> 2) & 3, t & 3, 3, sg & 3);
    d.m0 = t < 112 ? d.m0 : NOLDS; d.so = (unsigned)t < 112u ? d.so : OOR;
    return d;
  };
  asm volatile("s_barrier" ::: "memory");
  dma_all(mkWq(0, role, role, 3, true));
  s16x4 fh[10][2];
  auto FA = [&](int f) { return cat(fh[f][0], fh[f][1]); };
  auto rdT = [&](int f, int h, unsigned tb, unsigned off, int ct) { fh[f][h] = trd((tb ^ (32u * ct)) + (off + 4096 * h)); };
  auto rdQ = [&](int f, int h, unsigned tb, unsigned off, int ct) { fh[f][h] = trd((tb ^ (32u * ct)) + (off + 1024 * h)); };
  auto nop = [&](int, int) {};
  auto zfa = [&]() {
#pragma unroll
    for (int i = 0; i < 10; ++i) fh[i][0] = fh[i][1] = (s16x4){0, 0, 0, 0};
  };
  auto prologue = [&](bf16x8 (*xf)[2], const int R, auto extra) {
    waitbar<4>();
    {
      const Dma f4 = mkWq(0, 4 + R, R, 0);
      const unsigned xa = ldsb + 2 * qh * REG + (1 + (r >> 3)) * SLOT + (r & 7) * 1024 + 16 * (g ^ r);
#pragma unroll
      for (int i = 0; i < 16; ++i) {
        xf[i][0] = ldsr128((xa ^ (64u * (i & 3))) + 256 * (i >> 2));
        xf[i][1] = ldsr128((xa ^ (64u * (i & 3))) + 256 * (i >> 2) + REG);
        dma_slot<3, 4>(f4, i);
      }
    }
    waitbar<4>();
    f32x4 q0[2][2] = {{zero4(), zero4()}, {zero4(), zero4()}};
    unsigned tqh = tq + 2 * REG;
    PIN(tqh);
    auto mm0 = [&](int j, int m, int t) { q0[m & 1][t] = mfma(FA(m), xf[4 * j + (m >> 1)][t], q0[m & 1][t]); };
#pragma unroll
    for (int j = 0; j < 4; ++j) {
      if (j == 0) { dma_all(mkWq(0, 8 + R, R, 1)); dma_all(mkWq(0, 12 + R, R, 2)); }
      if (j >= 1) { const int sg = j - 2; dma_all(R == 0 ? stQ(sg) : R == 1 ? stK(sg) : R == 2 ? stV(sg) : stO(sg)); }
      pstep<8, 3, 2>([&](int m, int h) { rdQ(m, h, m < 4 ? tq : tqh, ((m >> 1) & 1) * REG + ((j + 3) & 3) * SLOT, 2 * R + (m & 1)); },
                     [&](int m, int t) { if (j > 0) mm0(j - 1, m, t); }, [&](int m, int t) { mm0(j, m, t); }, nop);
      if (j == 0) extra();
      if (R == 2) { if (j < 3) waitbar<25>(); else bar(); } else waitbar<8>();
    }
#pragma unroll
    for (int m = 10; m < 16; ++m) mm0(3, m >> 1, m & 1);
    ldsw128(xb + 2048 * R + (R == 3 ? 2048 : 0), swap16(pack8(q0[0][0] * SC, q0[1][0] * SC)));
    ldsw128(xb + 2048 * R + (R == 3 ? 2048 : 0) + 1024, swap16(pack8(q0[0][1] * SC, q0[1][1] * SC)));
    SBAR();
  };
  auto noextra = [&]() {};
  const rsrc_t sP = __builtin_amdgcn_make_buffer_rsrc((void*)ws, (short)0, WS_FLAG, 0x00020000);

  if (role == 0) {
    setup(true);
    bf16x8 xf[16][2];
    prologue(xf, 0, noextra);
    f32x4 qacc[8][2];
#pragma unroll
    for (int i = 0; i < 8; ++i) qacc[i][0] = qacc[i][1] = zero4();
    auto mmQ = [&](int i, int m, int t) { qacc[m][t] = mfma(FA(m), xf[i][t], i == 0 ? zero4() : qacc[m][t]); };
    auto putQ = [&](int k) {
      ldsw128(xb + 1024 * (k < 6 ? k : k + 2), swap16(pack8(qacc[k & 6][k & 1] * SC, qacc[(k & 6) + 1][k & 1] * SC)));
    };
    for (int u = 1; u < 8; ++u) {
#pragma unroll
      for (int i = 0; i < 16; ++i) {
        const Dma da = stQ(16 * (u - 1) + i + 2);
        pstep<8, D1, AH>([&](int m, int h) { rdQ(m, h, tq, ((i + 3) & 3) * SLOT, m); },
                         [&](int m, int t) { mmQ((i + 15) & 15, m, t); }, [&](int m, int t) { mmQ(i, m, t); },
                         [&](int k, int h) { if (h == 0 && k == 0) dma_all(da); if (h == 1 && i == 0 && u > 1) putQ(k); });
        waitbar<8>();
      }
    }
#pragma unroll
    for (int m = 8 - D1; m < 8; ++m) { mmQ(15, m, 0); mmQ(15, m, 1); }
#pragma unroll
    for (int k = 0; k < 8; ++k) putQ(k);
    bar();
#pragma unroll
    for (int j = 0; j < 4; ++j) { dma_all(mkWo(7, 0, j + 1, 0, j)); bar(); bar(); bar(); bar(); }
    dma_all(mkWo(7, 1, 1, 1, 0)); dma_all(mkWo(7, 1, 2, 1, 1)); bar();
    dma_all(mkWo(7, 2, 1, 2, 0)); dma_all(mkWo(7, 3, 1, 3, 0)); dma_all(mkWo(7, 1, 3, 1, 2)); bar();
    dma_all(mkWo(7, 2, 2, 2, 1)); dma_all(mkWo(7, 3, 2, 3, 1)); dma_all(mkWo(7, 1, 0, 1, 3)); bar();
    dma_all(mkWo(7, 2, 3, 2, 2)); dma_all(mkWo(7, 3, 3, 3, 2)); dma_all(mkWo(7, 2, 0, 2, 3)); waitbar<28>();
    dma_all(mkWo(7, 3, 0, 3, 3)); waitbar<20>();
    waitbar<8>();
    waitbar<0>();
    bar();
  } else if (role == 1) {
    setup(true);
    { bf16x8 xf[16][2]; prologue(xf, 1, noextra); }
    dma_all(stK(2));
    const unsigned sig = ((g & 1) << 1) | (g >> 1);
    const unsigned kb = ldsb + REG + r * 256 + 16 * (sig ^ r);
    unsigned pb = ldsb + PB + qh * 3072 + lane * 16;
    PIN(pb);
    f32x4 sb[4][2][2], negm[2] = {zero4(), zero4()};
    bf16x8 qf[4][2], fa[8];
#pragma unroll
    for (int i = 0; i < 16; ++i) sb[i >> 2][(i >> 1) & 1][i & 1] = zero4();
#pragma unroll
    for (int i = 0; i < 8; ++i) { qf[i >> 1][i & 1] = zero8(); fa[i] = zero8(); }
    float mx[2] = {0.f, 0.f}, mc = 0.f;
    unsigned flag = 0;
    auto mmK = [&](int i, int m, int t) {
      sb[i & 3][m >> 2][t] = mfma(fa[m], qf[m & 3][t], (m & 3) == 0 ? (i < 2 ? zero4() : negm[t]) : sb[i & 3][m >> 2][t]);
    };
    auto soft = [&](int i, int k, int h, bool chk) {
      const int je = (i + 14) & 15, jm = (i + 15) & 15;
      auto& se = sb[je & 3];
      auto& sm = sb[jm & 3];
      auto g0 = [](int q) { return q < 4 ? 3 * q : 12 + 2 * (q - 4); };
      auto gn = [](int q) { return q < 4 ? 3 : 2; };
      if (h == 0 && k < 6) {
#pragma unroll
        for (int e = g0(k); e < g0(k) + gn(k); ++e) se[(e >> 2) & 1][e >> 3][e & 3] = __builtin_amdgcn_exp2f(se[(e >> 2) & 1][e >> 3][e & 3]);
      }
      if (h == 0) return;
      if (k == 3) { u32x4 w = pack8(se[0][0], se[1][0]); w[0] |= flag; ldsw128(pb + (je & 1) * 6144, w); }
      if (k == 6) ldsw128(pb + (je & 1) * 6144 + 1024, pack8(se[0][1], se[1][1]));
      if (chk) {
        if (k == 3) { mx[0] = max3f(sm[0][0][0], sm[0][0][1], sm[0][0][2]); mx[1] = max3f(sm[0][1][0], sm[0][1][1], sm[0][1][2]); PIN(mx[0]); PIN(mx[1]); }
        if (k == 4) { mx[0] = max3f(mx[0], sm[0][0][3], sm[1][0][0]); mx[1] = max3f(mx[1], sm[0][1][3], sm[1][1][0]); PIN(mx[0]); PIN(mx[1]); }
        if (k == 5) { mx[0] = max3f(mx[0], sm[1][0][1], sm[1][0][2]); mx[1] = max3f(mx[1], sm[1][1][1], sm[1][1][2]); PIN(mx[0]); PIN(mx[1]); }
        if (k == 6 && jm != 0) { mc = max3f(sm[1][0][3], sm[1][1][3], mx[0]); PIN(mc); mc = max3f(mc, mx[1], 0.f); }
        if (k == 7) {
          flag = 0;
          if (jm == 0) {
#pragma unroll
            for (int t = 0; t < 2; ++t) {
              const float d = red_max(__builtin_fmaxf(mx[t], sm[1][t][3]));
              sm[0][t] -= d; sm[1][t] -= d; sb[i & 3][0][t] -= d; sb[i & 3][1][t] -= d;
              negm[t] = (f32x4){-d, -d, -d, -d};
            }
          } else if (__builtin_expect(__builtin_amdgcn_ballot_w64(mc > THR) != 0, 0)) {
            flag = 0x80000000u;
            f32x2 al;
#pragma unroll
            for (int t = 0; t < 2; ++t) {
              const float d = __builtin_fmaxf(red_max(__builtin_fmaxf(mx[t], sm[1][t][3])), 0.f);
              al[t] = __builtin_amdgcn_exp2f(-d);
              sm[0][t] -= d; sm[1][t] -= d;
              if (jm != 15) { sb[i & 3][0][t] -= d; sb[i & 3][1][t] -= d; negm[t] -= d; }
            }
            *(LAS f32x2*)(size_t)(pb + (jm & 1) * 6144 + 2048 + 8 * ((jm >> 1) & 1)) = al;
          }
        }
      }
    };
#pragma unroll
    for (int i = 0; i < 16; ++i) PIN(sb[i >> 2][(i >> 1) & 1][i & 1]);
#pragma unroll
    for (int i = 0; i < 8; ++i) { PIN(qf[i >> 1][i & 1]); PIN(fa[i]); }
    waitbar<8>();
    for (int u = 0; u < 8; ++u) {
#pragma unroll
      for (int i = 0; i < 16; ++i) {
        const Dma db = stK(16 * u + i + 3);
        pstep<8, 3, AH>([&](int m, int h) { if (h == 0) fa[m] = ldsr128((kb ^ (64u * (m & 3))) + ((i & 3) * SLOT + 4096 * (m >> 2))); },
                        [&](int m, int t) { mmK((i + 15) & 15, m, t); }, [&](int m, int t) { mmK(i, m, t); },
                        [&](int k, int h) {
                          if (h == 1 && i == 0 && k < 3) {
                            qf[k + 1][0] = ldsr128(xb + 2048 * (k < 2 ? k + 1 : 4)); qf[k + 1][1] = ldsr128(xb + 2048 * (k < 2 ? k + 1 : 4) + 1024);
                          }
                          if (h == 1 && k == 2) dma_all(db);
                          soft(i, k, h, true);
                        },
                        [&]() { if (i == 0) { qf[0][0] = ldsr128(xb); qf[0][1] = ldsr128(xb + 1024); } });
        waitbar<8>();
      }
    }
#pragma unroll
    for (int m = 10; m < 16; ++m) mmK(15, m >> 1, m & 1);
#pragma unroll
    for (int k = 0; k < 16; ++k) soft(0, k >> 1, k & 1, true);
    bar();
#pragma unroll
    for (int k = 0; k < 16; ++k) soft(1, k >> 1, k & 1, false);
    bar();
    bar(); bar(); bar(); bar(); bar();
    bar();
  } else {
    const unsigned grp = blockIdx.x & ~3u;
    auto stP = [&](unsigned ln, f32x4 a0, f32x4 a1, int gi, int ct) {
      const unsigned so = ((grp + ((hg + gi) & 3)) * 3 + gi - 1) * 16384 + qh * 8192 + (ct >> 2) * 4096;
      __builtin_amdgcn_raw_buffer_store_b128(pack8(a0, a1), sP, ln * 16 + (ct & 3) * 1024, so, 16);
    };
    auto putF = [&](unsigned ln, f32x4 a, int n) { ldsw128(ldsb + (n >> 2) * REG + qh * SLOT + (n & 3) * 1024 + ln * 16, __builtin_bit_cast(u32x4, a)); };
    if (role == 2) {
      setup(true);
#pragma unroll
      for (int i = 0; i < 10; ++i) ldsw128(xb + 1024 * i, (u32x4){0, 0, 0, 0});
      GAS unsigned long long* ep = (GAS unsigned long long*)(ws + WS_EPOCH + (blockIdx.x >> 2) * 128);
      unsigned long long tag;
      u32x4 wm[16];
      {
        bf16x8 xf[16][2];
        prologue(xf, 2, [&]() {
          asm volatile("global_load_dwordx2 %0, %1, off sc1" : "=&v"(tag) : "v"(ep) : "memory");
#pragma unroll
          for (int i = 0; i < 16; ++i) {
            const unsigned so = ((8 * hg + head(0)) * 128 + 64 * qh + 4 * i) * 1024 + ((rot + hg) & 3) * 256;
            asm volatile("buffer_load_dwordx4 %0, %1, %2, %3 offen" : "=&v"(wm[i]) : "v"(dO), "s"(sO), "s"(so) : "memory");
          }
        });
      }
      bar();
      dma_all(stV(3));
      asm volatile("s_waitcnt vmcnt(4) lgkmcnt(0)\n\ts_barrier"
                   : "+v"(tag), "+v"(wm[0]), "+v"(wm[1]), "+v"(wm[2]), "+v"(wm[3]), "+v"(wm[4]), "+v"(wm[5]), "+v"(wm[6]), "+v"(wm[7]), "+v"(wm[8]), "+v"(wm[9]),
                     "+v"(wm[10]), "+v"(wm[11]), "+v"(wm[12]), "+v"(wm[13]), "+v"(wm[14]), "+v"(wm[15])::"memory");
      tag = ((unsigned long long)rfl((unsigned)(tag >> 32)) << 32) | rfl((unsigned)tag);
      tag = (tag ^ (tag >> 31)) * 0x9E3779B97F4A7C15ull + 0x632BE59BD9B4E019ull;
      unsigned pb = ldsb + PB + qh * 3072 + lane * 16;
      PIN(pb);
      f32x4 o[8][2], oa[4][2][2];
      bf16x8 pf[2][2], of[2][2];
#pragma unroll
      for (int i = 0; i < 16; ++i) { o[i >> 1][i & 1] = zero4(); oa[i >> 2][(i >> 1) & 1][i & 1] = zero4(); }
      zfa();
      pf[0][0] = pf[0][1] = pf[1][0] = pf[1][1] = of[0][0] = of[0][1] = of[1][0] = of[1][1] = zero8();
      f32x2 linv = {0.f, 0.f};
      f32x4 lacc[2] = {zero4(), zero4()};
      bf16x8 ones = {0x3f80, 0x3f80, 0x3f80, 0x3f80, 0x3f80, 0x3f80, 0x3f80, 0x3f80};
      PIN(ones);
      auto mm3 = [&](int j, int m, int t) {
        if (m < 8) o[m][t] = mfma(FA(m), pf[j & 1][t], j == 0 ? zero4() : o[m][t]);
        else { const int iw = (j + 15) & 15; oa[iw & 3][m & 1][t] = mfma(FA(m), of[(iw >> 2) & 1][t], oa[iw & 3][m & 1][t]); }
      };
      auto packO = [&](int k) { return pack8(o[k & 6][k & 1] * linv[k & 1], o[(k & 6) + 1][k & 1] * linv[k & 1]); };
      dma_all(stV(4));
#pragma unroll
      for (int i = 0; i < 16; ++i) { PIN(o[i >> 1][i & 1]); PIN(oa[i >> 2][(i >> 1) & 1][i & 1]); }
      bar();
      dma_all(stV(5)); waitbar<8>();
      for (int u = 0; u < 8; ++u) {
#pragma unroll
        for (int j = 0; j < 16; ++j) {
          const int iw = (j + 15) & 15;
          const Dma da = stV(16 * u + 6 + j);
          pstep<10, D3, AH>([&](int m, int h) { if (m < 8) rdT(m, h, tbh, ((j + 3) & 3) * SLOT, m); else rdT(m, h, tbh, REG + ((j + 3) & 3) * SLOT, m - 2); },
                            [&](int m, int t) { mm3((j + 15) & 15, m, t); }, [&](int m, int t) { mm3(j, m, t); },
                            [&](int k, int h) {
                              if (h == 0) { if (k == 5) dma_all(da); return; }
                              if (j == 0 && k < 8) ldsw128(xb + 1024 * k, packO(k));
                              if (j > 0 && k == D3 - 1) {
                                if (__builtin_expect(__builtin_amdgcn_ballot_w64((int)__builtin_bit_cast(u32x4, pf[j & 1][0])[0] < 0) != 0, 0)) {
                                  const f32x2 alpha = *(const LAS f32x2*)(size_t)(pb + (j & 1) * 6144 + 2048 + 8 * ((j >> 1) & 1));
                                  { u32x4 w = __builtin_bit_cast(u32x4, pf[j & 1][0]); w[0] &= 0x7fffffffu; pf[j & 1][0] = __builtin_bit_cast(bf16x8, w); }
                                  f32x4 a0 = {alpha[0], alpha[0], alpha[0], alpha[0]}, a1 = {alpha[1], alpha[1], alpha[1], alpha[1]};
                                  PIN(a0); PIN(a1);
#pragma unroll
                                  for (int ct = 0; ct < 8; ++ct) { o[ct][0] *= a0; o[ct][1] *= a1; }
                                  lacc[0] *= a0; lacc[1] *= a1;
                                }
                              }
                              if (k == 4 || k == 6) lacc[k == 6] = mfma(ones, pf[j & 1][k == 6], j == 0 ? zero4() : lacc[k == 6]);
                              if (j == 15 && k == 9) linv = (f32x2){1.0f / lacc[0][0], 1.0f / lacc[1][0]};
                              if (k == 8 && (iw & 3) == 0) { of[(iw >> 2) & 1][0] = ldsr128(xb + 2048 * (iw >> 2)); of[(iw >> 2) & 1][1] = ldsr128(xb + 2048 * (iw >> 2) + 1024); }
                            },
                            [&]() {
                              pf[j & 1][0] = ldsr128(pb + (j & 1) * 6144); pf[j & 1][1] = ldsr128(pb + (j & 1) * 6144 + 1024);
                            });
          waitbar<8>();
        }
      }
#pragma unroll
      for (int m = 10 - D3; m < 10; ++m) { mm3(15, m, 0); mm3(15, m, 1); }
      rdT(8, 0, tbh, REG + 3 * SLOT, 6); rdT(8, 1, tbh, REG + 3 * SLOT, 6); rdT(9, 0, tbh, REG + 3 * SLOT, 7); rdT(9, 1, tbh, REG + 3 * SLOT, 7);
      bf16x8 og[8];
#pragma unroll
      for (int k = 0; k < 8; ++k) { const u32x4 w = packO(k); ldsw128(xb + 1024 * k, w); og[k] = __builtin_bit_cast(bf16x8, w); }
      mm3(0, 8, 0); mm3(0, 8, 1); mm3(0, 9, 0); mm3(0, 9, 1);
      bar();
      unsigned ln;
      asm volatile("v_mbcnt_lo_u32_b32 %0, -1, 0\n\tv_mbcnt_hi_u32_b32 %0, -1, %0" : "=v"(ln));
      const unsigned tl2 = tbh - 2 * REG;
#pragma unroll
      for (int j = 0; j < 4; ++j) {
        const int gi = (j + 1) & 3;
#pragma unroll
        for (int m = 0; m < 16; ++m) rdT(m >> 1, m & 1, m < 8 ? tl2 : tbh, ((m >> 2) & 1) * REG + j * SLOT, 6 + ((m >> 1) & 1));
#pragma unroll
        for (int m = 0; m < 8; ++m) {
          oa[gi][m & 1][0] = mfma(FA(m), og[m & 6], oa[gi][m & 1][0]);
          oa[gi][m & 1][1] = mfma(FA(m), og[(m & 6) + 1], oa[gi][m & 1][1]);
        }
        if (j < 3) {
          stP(ln, oa[gi][0][0], oa[gi][0][1], gi, 6); stP(ln, oa[gi][1][0], oa[gi][1][1], gi, 7);
          bar();
        }
      }
#pragma unroll
      for (int q = 0; q < 4; ++q) putF(ln, oa[0][q >> 1][q & 1], 12 + q);
      waitbar<0>();
      if (qh == 0) {
        if (ln < 3)
          __hip_atomic_store((GAS unsigned long long*)(ws + WS_FLAG + ((grp + ((hg + ln + 1) & 3)) * 4 + ln) * 128), tag, __ATOMIC_RELAXED, __HIP_MEMORY_SCOPE_AGENT);
        GAS unsigned long long* fl = (GAS unsigned long long*)(ws + WS_FLAG + (blockIdx.x * 4 + (ln < 3 ? ln : 0)) * 128);
        for (unsigned spins = 0; spins < (1u << 22); ++spins) {
          const unsigned long long v = __hip_atomic_load(fl, __ATOMIC_RELAXED, __HIP_MEMORY_SCOPE_AGENT);
          if (__all(v == tag)) break;
          __builtin_amdgcn_s_sleep(1);
        }
        if (hg == 0 && ln == 0) __hip_atomic_store(ep, tag, __ATOMIC_RELAXED, __HIP_MEMORY_SCOPE_AGENT);
      }
    } else {
      setup(true);
      { bf16x8 xf[16][2]; prologue(xf, 3, noextra); }
      dma_all(stO(2)); waitbar<8>();
      f32x4 oa[4][6][2];
      bf16x8 of[2][2];
#pragma unroll
      for (int i = 0; i < 48; ++i) oa[i / 12][(i % 12) >> 1][i & 1] = zero4();
      zfa();
      of[0][0] = of[0][1] = of[1][0] = of[1][1] = zero8();
      auto mm4 = [&](int nq, int par, int m, int t) { oa[nq][m][t] = mfma(FA(m), of[par][t], oa[nq][m][t]); };
#pragma unroll
      for (int i = 0; i < 48; ++i) PIN(oa[i / 12][(i % 12) >> 1][i & 1]);
      for (int s = 0; s < 17; ++s) waitbar<0>();
      dma_all(stO(20)); bar(); dma_all(stO(21)); bar(); dma_all(stO(22)); waitbar<8>();
      for (int u = 0; u < 7; ++u) {
#pragma unroll
        for (int i = 0; i < 16; ++i) {
          const int ip = (i + 15) & 15;
          const Dma db = stO(16 * u + 23 + i);
          pstep<6, D4, AH>([&](int m, int h) { rdT(m, h, tbh, REG + (i & 3) * SLOT, m); },
                           [&](int m, int t) { mm4(ip & 3, (ip >> 2) & 1, m, t); }, [&](int m, int t) { mm4(i & 3, (i >> 2) & 1, m, t); },
                           [&](int k, int h) { if (h == 0 && k == 2) dma_all(db); },
                           [&]() { if ((i & 3) == 0) { of[(i >> 2) & 1][0] = ldsr128(xb + 2048 * (i >> 2)); of[(i >> 2) & 1][1] = ldsr128(xb + 2048 * (i >> 2) + 1024); } });
          waitbar<8>();
        }
      }
      unsigned ln;
      asm volatile("v_mbcnt_lo_u32_b32 %0, -1, 0\n\tv_mbcnt_hi_u32_b32 %0, -1, %0" : "=v"(ln));
      const unsigned tl2 = tbh - 2 * REG;
#pragma unroll
      for (int j = 0; j < 4; ++j) {
        const int gi = (j + 1) & 3;
#pragma unroll
        for (int q = 0; q < 4; ++q) {
          pstep<6, D4, AH>([&](int m, int h) { rdT(m, h, q < 2 ? tl2 : tbh, (q & 1) * REG + j * SLOT, m); },
                           [&](int m, int t) { if (q > 0) mm4(gi, (q - 1) & 1, m, t); else mm4(j == 0 ? 3 : j, 1, m, t); },
                           [&](int m, int t) { mm4(gi, q & 1, m, t); },
                           [&](int k, int h) {
                             if (h == 1 && j > 0 && q == 0) stP(ln, oa[j][k][0], oa[j][k][1], j, k);
                           },
                           [&]() { of[q & 1][0] = ldsr128(xb + 2048 * q); of[q & 1][1] = ldsr128(xb + 2048 * q + 1024); });
          if (j == 3 && q == 1) waitbar<0>();
        }
        if (j < 3) bar();
      }
#pragma unroll
      for (int m = 6 - D4; m < 6; ++m) { mm4(0, 1, m, 0); mm4(0, 1, m, 1); }
#pragma unroll
      for (int n = 0; n < 12; ++n) putF(ln, oa[0][n >> 1][n & 1], n);
    }
  }
  waitbar<0>();
  {
    unsigned ln;
    asm volatile("v_mbcnt_lo_u32_b32 %0, -1, 0\n\tv_mbcnt_hi_u32_b32 %0, -1, %0" : "=v"(ln));
    f32x4 a[4];
    u32x4 in[6];
#pragma unroll
    for (int j = 0; j < 3; ++j)
#pragma unroll
      for (int c = 0; c < 2; ++c)
        in[2 * j + c] = __builtin_amdgcn_raw_buffer_load_b128(sP, ln * 16 + c * 1024, (blockIdx.x * 3 + j) * 16384 + qh * 8192 + role * 2048, 16);
#pragma unroll
    for (int q = 0; q < 4; ++q) {
      a[q] = __builtin_bit_cast(f32x4, ldsr128(ldsb + role * REG + qh * SLOT + q * 1024 + ln * 16));
#pragma unroll
      for (int j = 0; j < 3; ++j) {
        const unsigned w0 = in[2 * j + (q >> 1)][2 * (q & 1)], w1 = in[2 * j + (q >> 1)][2 * (q & 1) + 1];
        a[q] += (f32x4){__uint_as_float(w0 << 16), __uint_as_float(w0 & 0xffff0000u), __uint_as_float(w1 << 16), __uint_as_float(w1 & 0xffff0000u)};
      }
    }
    char* orow = out + ((size_t)b * 64 + 32 * qh + (ln & 15)) * 2048 + p * 1024 + ((rot + hg) & 3) * 256 + role * 64 + ((ln & 16) * 2 + (ln >> 5) * 16);
    *(u32x4*)(orow) = swap16(pack8(a[0], a[2]));
    *(u32x4*)(orow + 16 * 2048) = swap16(pack8(a[1], a[3]));
  }
}

extern "C" void kernel_launch(void* const* d_in, const int* in_sizes, int n_in, void* d_out, int out_size, void* d_ws, size_t ws_size,
                              hipStream_t stream) {
  (void)in_sizes; (void)n_in; (void)out_size; (void)ws_size;
  (void)hipFuncSetAttribute((const void*)mha_fused, hipFuncAttributeMaxDynamicSharedMemorySize, LDS_BYTES);
  mha_fused<<<256, 512, LDS_BYTES, stream>>>((const char*)d_in[0], (const char*)d_in[1], (const char*)d_in[2], (const char*)d_in[3],
                                             (const char*)d_in[4], (const char*)d_in[5], (const char*)d_in[6], (const char*)d_in[7],
                                             (const char*)d_in[8], (const char*)d_in[9], (char*)d_ws, (char*)d_out);
}
```
